```python
import torch

def _softmax(x, axis=-1):
    return torch.softmax(x, dim=axis)
def _gelu(x):
    return torch.nn.functional.gelu(x, approximate="tanh")
def _silu(x):
    return torch.nn.functional.silu(x)
def _sigmoid(x):
    return torch.sigmoid(x)
def _layernorm(x, axis=-1, eps=1e-5):
    m = x.mean(dim=axis, keepdim=True)
    v = x.var(dim=axis, keepdim=True, unbiased=False)
    return (x - m) / torch.sqrt(v + eps)
def _scatter_add(tgt, idx, src, axis=0):
    return tgt.clone().index_add_(axis, idx, src)
def _segment_sum(src, seg, n):
    out = torch.zeros((n,) + src.shape[1:], dtype=src.dtype)
    return out.index_add_(0, seg, src)
def _pad(x, pw):
    flat = tuple(v for pair in reversed(pw) for v in pair)
    return torch.nn.functional.pad(x, flat)

kernel_name = 'gated_mix__compressed_kv_cla__swiglu_ffn__decode'

def setup_inputs():

    _w = (lambda a: (lambda t: t.to(torch.bfloat16) if t.is_floating_point() else t)(torch.as_tensor(a)))
    def _A_setup():
        B, H, Sq, Sk, D, M = 4, 32, 1, 4096, 128, 512
        Dc = 4 * D
        return {
            "x": _w((lambda *s: torch.randn(*s, dtype=torch.bfloat16) * 0.05)(B, Sq, M)),
            "c_kv": _w((lambda *s: torch.randn(*s, dtype=torch.bfloat16) * 0.8)(B, Sk, Dc)),
            "w_q": _w((lambda *s: torch.randn(*s, dtype=torch.bfloat16) * 6.4)(M, H, D) * (M ** -0.5)),
            "w_uk": _w((lambda *s: torch.randn(*s, dtype=torch.bfloat16) * 6.4)(Dc, H, D) * (Dc ** -0.5)),
            "w_uv": _w((lambda *s: torch.randn(*s, dtype=torch.bfloat16) * 0.8)(Dc, H, D) * (Dc ** -0.5)),
            "w_o": _w((lambda *s: torch.randn(*s, dtype=torch.bfloat16) * 0.8)(H, D, M) * ((H * D) ** -0.5)),
        }
    _A_in = _A_setup()

    def _B_setup():
        B, S, D, F = 4, 1, 512, 1792
        return {
            "x": _w((lambda *s: torch.randn(*s, dtype=torch.bfloat16) * 0.05)(B, S, D)),
            "w_gate": _w((lambda *s: torch.randn(*s, dtype=torch.bfloat16) * 0.1)(D, F)),
            "w_up": _w((lambda *s: torch.randn(*s, dtype=torch.bfloat16) * 0.1)(D, F)),
            "w_down": _w((lambda *s: torch.randn(*s, dtype=torch.bfloat16) * 0.1)(F, D)),
        }
    _B_in = _B_setup()

    return {
        "A_x": _A_in["x"],
        "A_c_kv": _A_in["c_kv"],
        "A_w_q": _A_in["w_q"],
        "A_w_uk": _A_in["w_uk"],
        "A_w_uv": _A_in["w_uv"],
        "A_w_o": _A_in["w_o"],
        "B_x": _B_in["x"],
        "B_w_gate": _B_in["w_gate"],
        "B_w_up": _B_in["w_up"],
        "B_w_down": _B_in["w_down"],
        "gate_w": _w((lambda *s: torch.randn(*s, dtype=torch.bfloat16) * 0.05)(512) * 16.0),
    }

def reference(A_x, A_c_kv, A_w_q, A_w_uk, A_w_uv, A_w_o, B_x, B_w_gate, B_w_up, B_w_down, gate_w):
    def _A_ref(x, c_kv, w_q, w_uk, w_uv, w_o):
        B, Sq, M = x.shape[0], x.shape[1], x.shape[2]
        Sk = c_kv.shape[1]
        H, D = w_q.shape[1], w_q.shape[2]
        q = torch.einsum("bqm,mhd->bhqd", x, w_q)
        k = torch.einsum("bkc,chd->bhkd", c_kv, w_uk)
        v = torch.einsum("bkc,chd->bhkd", c_kv, w_uv)
        scores = torch.einsum("bhqd,bhkd->bhqk", q, k) * (D ** -0.5)
        off = Sk - Sq
        qi = torch.arange(Sq)[:, None]
        kj = torch.arange(Sk)[None, :]
        cmask = kj <= (qi + off)
        scores = torch.where(cmask[None, None], scores, -1e9)
        p = _softmax(scores, axis=-1)
        ctx = torch.einsum("bhqk,bhkd->bhqd", p, v)
        return torch.einsum("bhqd,hdm->bqm", ctx, w_o)

    def _B_ref(x, w_gate, w_up, w_down):
        g = _silu(x @ w_gate)
        u = x @ w_up
        return (g * u) @ w_down

    x = A_x
    g = _sigmoid((x * gate_w).sum(-1, keepdims=True))
    a = _A_ref(x, A_c_kv, A_w_q, A_w_uk, A_w_uv, A_w_o)
    b = _B_ref(x, B_w_gate, B_w_up, B_w_down)
    return g * a + (1 - g) * b
```

```cpp
#include <hip/hip_runtime.h>
#include <hip/hip_bf16.h>
typedef unsigned short u16;
__device__ __forceinline__ float bf2f(u16 v){ return __uint_as_float(((unsigned)v)<<16); }
__device__ __forceinline__ u16 f2bf(float f){ unsigned u=__float_as_uint(f); u += 0x7fffu + ((u>>16)&1u); return (u16)(u>>16);}
__device__ __forceinline__ float rbf(float f){ return bf2f(f2bf(f)); }
constexpr int B=4,H=32,SK=4096,D=128,M=512,DC=512,F=1792;
__global__ void k1(const u16* x,const u16* wq,const u16* wg,const u16* wu,float* q,float* hb){
  int n = blockIdx.x*blockDim.x+threadIdx.x;
  if(n<H*D){
    float a[B]={0,0,0,0};
    for(int m=0;m<M;m++){ float w=bf2f(wq[(size_t)m*H*D+n]);
      for(int b=0;b<B;b++) a[b]+=bf2f(x[b*M+m])*w; }
    for(int b=0;b<B;b++) q[b*H*D+n]=rbf(a[b]);
  } else if(n<H*D+F){
    int f=n-H*D; float g[B]={0,0,0,0},u[B]={0,0,0,0};
    for(int m=0;m<M;m++){ float w1=bf2f(wg[(size_t)m*F+f]), w2=bf2f(wu[(size_t)m*F+f]);
      for(int b=0;b<B;b++){ float xv=bf2f(x[b*M+m]); g[b]+=xv*w1; u[b]+=xv*w2; } }
    for(int b=0;b<B;b++){ float gg=rbf(g[b]); float s=rbf(gg/(1.f+__expf(-gg))); hb[b*F+f]=rbf(s*rbf(u[b])); }
  }
}
__global__ void k2(const float* q,const u16* wuk,float* qa){
  int n = blockIdx.x*blockDim.x+threadIdx.x;
  int h=n/DC,c=n%DC; float a[B]={0,0,0,0};
  for(int d=0;d<D;d++){ float w=bf2f(wuk[(size_t)c*H*D+h*D+d]); for(int b=0;b<B;b++) a[b]+=q[b*H*D+h*D+d]*w; }
  for(int b=0;b<B;b++) qa[(b*H+h)*DC+c]=a[b];
}
__global__ void k3(const float* qa,const u16* ckv,float* s){
  int k = blockIdx.x*blockDim.x+threadIdx.x; int h=blockIdx.y,b=blockIdx.z;
  float a=0; const u16* cp=ckv+((size_t)b*SK+k)*DC; const float* qp=qa+(b*H+h)*DC;
  for(int c=0;c<DC;c++) a+=qp[c]*bf2f(cp[c]);
  s[(size_t)(b*H+h)*SK+k]=a*0.08838834764831845f;
}
__global__ void k4(const float* s,const u16* ckv,float* cc){
  int h=blockIdx.x,b=blockIdx.y; __shared__ float p[SK]; __shared__ float red[512];
  const float* sp=s+(size_t)(b*H+h)*SK; int t=threadIdx.x;
  float m=-1e30f; for(int k=t;k<SK;k+=512) m=fmaxf(m,sp[k]); red[t]=m; __syncthreads();
  for(int o=256;o>0;o>>=1){ if(t<o) red[t]=fmaxf(red[t],red[t+o]); __syncthreads(); } m=red[0]; __syncthreads();
  float l=0; for(int k=t;k<SK;k+=512){ float e=__expf(sp[k]-m); p[k]=e; l+=e; } red[t]=l; __syncthreads();
  for(int o=256;o>0;o>>=1){ if(t<o) red[t]+=red[t+o]; __syncthreads(); } l=red[0];
  float a=0; for(int k=0;k<SK;k++) a+=p[k]*bf2f(ckv[((size_t)b*SK+k)*DC+t]);
  cc[(b*H+h)*DC+t]=a/l;
}
__global__ void k5(const float* cc,const u16* wuv,float* ctx){
  int n = blockIdx.x*blockDim.x+threadIdx.x; int h=n/D; float a[B]={0,0,0,0};
  for(int c=0;c<DC;c++){ float w=bf2f(wuv[(size_t)c*H*D+n]); for(int b=0;b<B;b++) a[b]+=cc[(b*H+h)*DC+c]*w; }
  for(int b=0;b<B;b++) ctx[b*H*D+n]=rbf(a[b]);
}
__global__ void k6(const float* ctx,const float* hb,const u16* wo,const u16* wd,const u16* x,const u16* gw,u16* out){
  int m=blockIdx.x; int b=blockIdx.y; int t=threadIdx.x; __shared__ float r1[256],r2[256],r3[256];
  float a=0,f=0,g=0;
  for(int k=t;k<H*D;k+=256) a+=ctx[b*H*D+k]*bf2f(wo[(size_t)k*M+m]);
  for(int k=t;k<F;k+=256) f+=hb[b*F+k]*bf2f(wd[(size_t)k*M+m]);
  for(int k=t;k<M;k+=256) g+=rbf(bf2f(x[b*M+k])*bf2f(gw[k]));
  r1[t]=a;r2[t]=f;r3[t]=g;__syncthreads();
  for(int o=128;o>0;o>>=1){ if(t<o){r1[t]+=r1[t+o];r2[t]+=r2[t+o];r3[t]+=r3[t+o];} __syncthreads(); }
  if(t==0){ float gg=rbf(r3[0]); gg=rbf(1.f/(1.f+__expf(-gg))); float aa=rbf(r1[0]), ff=rbf(r2[0]);
    out[b*M+m]=f2bf(rbf(gg*aa)+rbf(rbf(1.f-gg)*ff)); }
}
extern "C" void kernel_launch(void* const* d_in, const int* in_sizes, int n_in, void* d_out, int out_size, void* d_ws, size_t ws_size, hipStream_t stream) {
  const u16* x=(const u16*)d_in[0]; const u16* ckv=(const u16*)d_in[1]; const u16* wq=(const u16*)d_in[2]; const u16* wuk=(const u16*)d_in[3];
  const u16* wuv=(const u16*)d_in[4]; const u16* wo=(const u16*)d_in[5]; const u16* wg=(const u16*)d_in[7]; const u16* wu=(const u16*)d_in[8]; const u16* wd=(const u16*)d_in[9]; const u16* gw=(const u16*)d_in[10];
  float* ws=(float*)d_ws; float* q=ws; float* hb=q+B*H*D; float* qa=hb+B*F; float* s=qa+B*H*DC; float* cc=s+(size_t)B*H*SK; float* ctx=cc+B*H*DC;
  k1<<<(H*D+F)/64,64,0,stream>>>(x,wq,wg,wu,q,hb);
  k2<<<H*DC/64,64,0,stream>>>(q,wuk,qa);
  k3<<<dim3(SK/64,H,B),64,0,stream>>>(qa,ckv,s);
  k4<<<dim3(H,B),512,0,stream>>>(s,ckv,cc);
  k5<<<H*D/64,64,0,stream>>>(cc,wuv,ctx);
  k6<<<dim3(M,B),256,0,stream>>>(ctx,hb,wo,wd,x,gw,(u16*)d_out);
}
```

```cpp
#define MODE 0
#define METRIC 2
#define AMP 0
#include <hip/hip_runtime.h>
#include <hip/hip_bf16.h>
typedef unsigned short u16;
__device__ __forceinline__ float bf2f(u16 v){ return __uint_as_float(((unsigned)v)<<16); }
__device__ __forceinline__ u16 f2bf(float f){ unsigned u=__float_as_uint(f); u += 0x7fffu + ((u>>16)&1u); return (u16)(u>>16);}
__device__ __forceinline__ float rbf(float f){ return bf2f(f2bf(f)); }
constexpr int B=4,H=32,SK=4096,D=128,M=512,DC=512,F=1792;
__global__ void k1(const u16* x,const u16* wq,const u16* wg,const u16* wu,float* q,float* hb){
  int n = blockIdx.x*blockDim.x+threadIdx.x;
  if(n<H*D){
    float a[B]={0,0,0,0};
    for(int m=0;m<M;m++){ float w=bf2f(wq[(size_t)m*H*D+n]);
      for(int b=0;b<B;b++) a[b]+=bf2f(x[b*M+m])*w; }
    for(int b=0;b<B;b++) q[b*H*D+n]=rbf(a[b]);
  } else if(n<H*D+F){
    int f=n-H*D; float g[B]={0,0,0,0},u[B]={0,0,0,0};
    for(int m=0;m<M;m++){ float w1=bf2f(wg[(size_t)m*F+f]), w2=bf2f(wu[(size_t)m*F+f]);
      for(int b=0;b<B;b++){ float xv=bf2f(x[b*M+m]); g[b]+=xv*w1; u[b]+=xv*w2; } }
    for(int b=0;b<B;b++){ float gg=rbf(g[b]); float s=rbf(gg/(1.f+__expf(-gg))); hb[b*F+f]=rbf(s*rbf(u[b])); }
  }
}
__global__ void k2(const float* q,const u16* wuk,float* qa){
  int n = blockIdx.x*blockDim.x+threadIdx.x;
  int h=n/DC,c=n%DC; float a[B]={0,0,0,0};
  for(int d=0;d<D;d++){ float w=bf2f(wuk[(size_t)c*H*D+h*D+d]); for(int b=0;b<B;b++) a[b]+=q[b*H*D+h*D+d]*w; }
  for(int b=0;b<B;b++) qa[(b*H+h)*DC+c]=a[b];
}
__global__ void k3(const float* qa,const u16* ckv,float* s){
  int k = blockIdx.x*blockDim.x+threadIdx.x; int h=blockIdx.y,b=blockIdx.z;
  float a=0; const u16* cp=ckv+((size_t)b*SK+k)*DC; const float* qp=qa+(b*H+h)*DC;
  for(int c=0;c<DC;c++) a+=qp[c]*bf2f(cp[c]);
  s[(size_t)(b*H+h)*SK+k]=a*0.08838834764831845f;
}
__global__ void k4(const float* s,const u16* ckv,float* cc){
  int h=blockIdx.x,b=blockIdx.y; __shared__ float p[SK]; __shared__ float red[512];
  const float* sp=s+(size_t)(b*H+h)*SK; int t=threadIdx.x;
  float m=-1e30f; for(int k=t;k<SK;k+=512) m=fmaxf(m,sp[k]); red[t]=m; __syncthreads();
  for(int o=256;o>0;o>>=1){ if(t<o) red[t]=fmaxf(red[t],red[t+o]); __syncthreads(); } m=red[0]; __syncthreads();
  float l=0; for(int k=t;k<SK;k+=512){ float e=__expf(sp[k]-m); p[k]=e; l+=e; } red[t]=l; __syncthreads();
  for(int o=256;o>0;o>>=1){ if(t<o) red[t]+=red[t+o]; __syncthreads(); } l=red[0];
  float a=0; for(int k=0;k<SK;k++) a+=p[k]*bf2f(ckv[((size_t)b*SK+k)*DC+t]);
  cc[(b*H+h)*DC+t]=a/l;
}
__global__ void k5(const float* cc,const u16* wuv,float* ctx){
  int n = blockIdx.x*blockDim.x+threadIdx.x; int h=n/D; float a[B]={0,0,0,0};
  for(int c=0;c<DC;c++){ float w=bf2f(wuv[(size_t)c*H*D+n]); for(int b=0;b<B;b++) a[b]+=cc[(b*H+h)*DC+c]*w; }
  for(int b=0;b<B;b++) ctx[b*H*D+n]=rbf(a[b]);
}
__global__ void k6(const float* ctx,const float* hb,const u16* wo,const u16* wd,const u16* x,const u16* gw,u16* out){
  int m=blockIdx.x; int b=blockIdx.y; int t=threadIdx.x; __shared__ float r1[256],r2[256],r3[256];
  float a=0,f=0,g=0;
  for(int k=t;k<H*D;k+=256) a+=ctx[b*H*D+k]*bf2f(wo[(size_t)k*M+m]);
  for(int k=t;k<F;k+=256) f+=hb[b*F+k]*bf2f(wd[(size_t)k*M+m]);
  for(int k=t;k<M;k+=256) g+=rbf(bf2f(x[b*M+k])*bf2f(gw[k]));
  r1[t]=a;r2[t]=f;r3[t]=g;__syncthreads();
  for(int o=128;o>0;o>>=1){ if(t<o){r1[t]+=r1[t+o];r2[t]+=r2[t+o];r3[t]+=r3[t+o];} __syncthreads(); }
  if(t==0){ float gg=rbf(r3[0]); gg=rbf(1.f/(1.f+__expf(-gg))); float aa=rbf(r1[0]), ff=rbf(r2[0]);
    out[b*M+m]=f2bf(rbf(gg*aa)+rbf(rbf(1.f-gg)*ff)); }
}

typedef unsigned v4u __attribute__((ext_vector_type(4)));
struct Ptrs { const v4u* p[8]; };
#define NT 0
#define NTHR 512
template<int N> __device__ __forceinline__ void ldchunk(const v4u* base, int wg, int tid, v4u (&r)[N]) {
  const v4u* p = base + (size_t)wg*N*NTHR + tid;
#pragma unroll
  for (int i=0;i<N;i++) r[i] = NT ? __builtin_nontemporal_load(p + i*NTHR) : p[i*NTHR];
}
template<int N> __device__ __forceinline__ unsigned fold(v4u (&r)[N]) { unsigned a=0;
#pragma unroll
  for (int i=0;i<N;i++) a ^= r[i].x ^ r[i].y ^ r[i].z ^ r[i].w; return a; }
__global__ void __launch_bounds__(NTHR) kstream(const v4u* ckv,const v4u* wq,const v4u* wuk,const v4u* wuv,const v4u* wo,const v4u* wg,const v4u* wu,const v4u* wd, unsigned long long* st, unsigned* sink){
  unsigned long long t0 = __builtin_amdgcn_s_memrealtime();
  int wgid = blockIdx.x, tid = threadIdx.x;
  constexpr int U = 512/NTHR*1;
  v4u a[1024/NTHR], b[1024/NTHR], c[4096/NTHR], d[1024/NTHR], e[1024/NTHR];
  unsigned acc=0;
  ldchunk(wq,wgid,tid,a); ldchunk(wuk,wgid,tid,b);
  unsigned long long t1=0;
  ldchunk(ckv,wgid,tid,c);
  ldchunk(wuv,wgid,tid,d); ldchunk(wo,wgid,tid,e);
  v4u f0,f1,f2; f0=f1=f2=v4u{0,0,0,0};
  { int ti = tid<448?tid:447; f0 = wg[wgid*448+ti]; f1 = wu[wgid*448+ti]; f2 = wd[wgid*448+ti]; }
  __builtin_amdgcn_sched_barrier(0);
  acc ^= fold(a) ^ fold(b);
  acc ^= fold(c);
  acc ^= fold(d) ^ fold(e); acc ^= f0.x^f0.y^f0.z^f0.w^f1.x^f1.y^f1.z^f1.w^f2.x^f2.y^f2.z^f2.w;
  sink[wgid*NTHR+tid]=acc;
  __syncthreads();
  unsigned long long t2 = __builtin_amdgcn_s_memrealtime();
  if (tid==0){ st[wgid*4]=t0; st[wgid*4+1]=t1; st[wgid*4+2]=t2; }
}
__global__ void kamp(const unsigned long long* st){
  __shared__ unsigned long long mn[256], mx[256];
  int t=threadIdx.x;
  unsigned long long a=st[t*4+0], b=st[t*4+METRIC];
  mn[t]=a; mx[t]=b; __syncthreads();
  if(t==0){ unsigned long long lo=~0ull, hi=0; for(int i=0;i<256;i++){ lo = mn[i]<lo?mn[i]:lo; hi = mx[i]>hi?mx[i]:hi; }
    unsigned long long d = (hi-lo); if (d>100000) d=100000; d*=AMP;
    unsigned long long s=__builtin_amdgcn_s_memrealtime(); while(__builtin_amdgcn_s_memrealtime()-s<d) __builtin_amdgcn_s_sleep(2); }
}

extern "C" void kernel_launch(void* const* d_in, const int* in_sizes, int n_in, void* d_out, int out_size, void* d_ws, size_t ws_size, hipStream_t stream) {
  const u16* x=(const u16*)d_in[0]; const u16* ckv=(const u16*)d_in[1]; const u16* wq=(const u16*)d_in[2]; const u16* wuk=(const u16*)d_in[3];
  const u16* wuv=(const u16*)d_in[4]; const u16* wo=(const u16*)d_in[5]; const u16* wg=(const u16*)d_in[7]; const u16* wu=(const u16*)d_in[8]; const u16* wd=(const u16*)d_in[9]; const u16* gw=(const u16*)d_in[10];
  float* ws=(float*)d_ws; float* q=ws; float* hb=q+B*H*D; float* qa=hb+B*F; float* s=qa+B*H*DC; float* cc=s+(size_t)B*H*SK; float* ctx=cc+B*H*DC;
  unsigned long long* st=(unsigned long long*)((char*)d_ws+(128u<<20)); unsigned* sink=(unsigned*)((char*)d_ws+(129u<<20));
  kstream<<<256,NTHR,0,stream>>>((const v4u*)ckv,(const v4u*)wq,(const v4u*)wuk,(const v4u*)wuv,(const v4u*)wo,(const v4u*)wg,(const v4u*)wu,(const v4u*)wd,st,sink);
  kamp<<<1,256,0,stream>>>(st);
  k1<<<(H*D+F)/64,64,0,stream>>>(x,wq,wg,wu,q,hb);
  k2<<<H*DC/64,64,0,stream>>>(q,wuk,qa);
  k3<<<dim3(SK/64,H,B),64,0,stream>>>(qa,ckv,s);
  k4<<<dim3(H,B),512,0,stream>>>(s,ckv,cc);
  k5<<<H*D/64,64,0,stream>>>(cc,wuv,ctx);
  k6<<<dim3(M,B),256,0,stream>>>(ctx,hb,wo,wd,x,gw,(u16*)d_out);
}
```

```cpp
#define FILL 63
#define AMP 0
#define USEMAX 0
#include <hip/hip_runtime.h>
#include <hip/hip_bf16.h>
typedef unsigned short u16;
__device__ __forceinline__ float bf2f(u16 v){ return __uint_as_float(((unsigned)v)<<16); }
__device__ __forceinline__ u16 f2bf(float f){ unsigned u=__float_as_uint(f); u += 0x7fffu + ((u>>16)&1u); return (u16)(u>>16);}
__device__ __forceinline__ float rbf(float f){ return bf2f(f2bf(f)); }
constexpr int B=4,H=32,SK=4096,D=128,M=512,DC=512,F=1792;
__global__ void k1(const u16* x,const u16* wq,const u16* wg,const u16* wu,float* q,float* hb){
  int n = blockIdx.x*blockDim.x+threadIdx.x;
  if(n<H*D){
    float a[B]={0,0,0,0};
    for(int m=0;m<M;m++){ float w=bf2f(wq[(size_t)m*H*D+n]);
      for(int b=0;b<B;b++) a[b]+=bf2f(x[b*M+m])*w; }
    for(int b=0;b<B;b++) q[b*H*D+n]=rbf(a[b]);
  } else if(n<H*D+F){
    int f=n-H*D; float g[B]={0,0,0,0},u[B]={0,0,0,0};
    for(int m=0;m<M;m++){ float w1=bf2f(wg[(size_t)m*F+f]), w2=bf2f(wu[(size_t)m*F+f]);
      for(int b=0;b<B;b++){ float xv=bf2f(x[b*M+m]); g[b]+=xv*w1; u[b]+=xv*w2; } }
    for(int b=0;b<B;b++){ float gg=rbf(g[b]); float s=rbf(gg/(1.f+__expf(-gg))); hb[b*F+f]=rbf(s*rbf(u[b])); }
  }
}
__global__ void k2(const float* q,const u16* wuk,float* qa){
  int n = blockIdx.x*blockDim.x+threadIdx.x;
  int h=n/DC,c=n%DC; float a[B]={0,0,0,0};
  for(int d=0;d<D;d++){ float w=bf2f(wuk[(size_t)c*H*D+h*D+d]); for(int b=0;b<B;b++) a[b]+=q[b*H*D+h*D+d]*w; }
  for(int b=0;b<B;b++) qa[(b*H+h)*DC+c]=a[b];
}
__global__ void k3(const float* qa,const u16* ckv,float* s){
  int k = blockIdx.x*blockDim.x+threadIdx.x; int h=blockIdx.y,b=blockIdx.z;
  float a=0; const u16* cp=ckv+((size_t)b*SK+k)*DC; const float* qp=qa+(b*H+h)*DC;
  for(int c=0;c<DC;c++) a+=qp[c]*bf2f(cp[c]);
  s[(size_t)(b*H+h)*SK+k]=a*0.08838834764831845f;
}
__global__ void k4(const float* s,const u16* ckv,float* cc){
  int h=blockIdx.x,b=blockIdx.y; __shared__ float p[SK]; __shared__ float red[512];
  const float* sp=s+(size_t)(b*H+h)*SK; int t=threadIdx.x;
  float m=-1e30f; for(int k=t;k<SK;k+=512) m=fmaxf(m,sp[k]); red[t]=m; __syncthreads();
  for(int o=256;o>0;o>>=1){ if(t<o) red[t]=fmaxf(red[t],red[t+o]); __syncthreads(); } m=red[0]; __syncthreads();
  float l=0; for(int k=t;k<SK;k+=512){ float e=__expf(sp[k]-m); p[k]=e; l+=e; } red[t]=l; __syncthreads();
  for(int o=256;o>0;o>>=1){ if(t<o) red[t]+=red[t+o]; __syncthreads(); } l=red[0];
  float a=0; for(int k=0;k<SK;k++) a+=p[k]*bf2f(ckv[((size_t)b*SK+k)*DC+t]);
  cc[(b*H+h)*DC+t]=a/l;
}
__global__ void k5(const float* cc,const u16* wuv,float* ctx){
  int n = blockIdx.x*blockDim.x+threadIdx.x; int h=n/D; float a[B]={0,0,0,0};
  for(int c=0;c<DC;c++){ float w=bf2f(wuv[(size_t)c*H*D+n]); for(int b=0;b<B;b++) a[b]+=cc[(b*H+h)*DC+c]*w; }
  for(int b=0;b<B;b++) ctx[b*H*D+n]=rbf(a[b]);
}
__global__ void k6(const float* ctx,const float* hb,const u16* wo,const u16* wd,const u16* x,const u16* gw,u16* out){
  int m=blockIdx.x; int b=blockIdx.y; int t=threadIdx.x; __shared__ float r1[256],r2[256],r3[256];
  float a=0,f=0,g=0;
  for(int k=t;k<H*D;k+=256) a+=ctx[b*H*D+k]*bf2f(wo[(size_t)k*M+m]);
  for(int k=t;k<F;k+=256) f+=hb[b*F+k]*bf2f(wd[(size_t)k*M+m]);
  for(int k=t;k<M;k+=256) g+=rbf(bf2f(x[b*M+k])*bf2f(gw[k]));
  r1[t]=a;r2[t]=f;r3[t]=g;__syncthreads();
  for(int o=128;o>0;o>>=1){ if(t<o){r1[t]+=r1[t+o];r2[t]+=r2[t+o];r3[t]+=r3[t+o];} __syncthreads(); }
  if(t==0){ float gg=rbf(r3[0]); gg=rbf(1.f/(1.f+__expf(-gg))); float aa=rbf(r1[0]), ff=rbf(r2[0]);
    out[b*M+m]=f2bf(rbf(gg*aa)+rbf(rbf(1.f-gg)*ff)); }
}

#define R2(x) x x
#define R4(x) R2(R2(x))
#define R8(x) R2(R4(x))
#define R32(x) R4(R8(x))
#define STAMP(t) asm volatile("s_memrealtime %0\n s_waitcnt lgkmcnt(0)" : "=s"(t) :: "memory")
__global__ void __launch_bounds__(64) kjump(unsigned long long* st){
  unsigned long long t0,t1; STAMP(t0);
  asm volatile(R32("s_branch 1f\n .fill %c0, 4, 0xBF800000\n 1:\n") :: "i"(FILL) : "memory");
  STAMP(t1);
  if (threadIdx.x==0) { st[blockIdx.x]=t1-t0; }
}
__global__ void kamp(const unsigned long long* st){
  if(threadIdx.x==0){ unsigned long long s=0, mx=0; for(int i=0;i<256;i++){ s+=st[i]; mx = st[i]>mx?st[i]:mx; }
    unsigned long long d = USEMAX ? mx*256 : s; if (d>25600000) d=25600000; d = d*AMP/256;
    unsigned long long s0=__builtin_amdgcn_s_memrealtime(); while(__builtin_amdgcn_s_memrealtime()-s0<d) __builtin_amdgcn_s_sleep(2); }
}

extern "C" void kernel_launch(void* const* d_in, const int* in_sizes, int n_in, void* d_out, int out_size, void* d_ws, size_t ws_size, hipStream_t stream) {
  const u16* x=(const u16*)d_in[0]; const u16* ckv=(const u16*)d_in[1]; const u16* wq=(const u16*)d_in[2]; const u16* wuk=(const u16*)d_in[3];
  const u16* wuv=(const u16*)d_in[4]; const u16* wo=(const u16*)d_in[5]; const u16* wg=(const u16*)d_in[7]; const u16* wu=(const u16*)d_in[8]; const u16* wd=(const u16*)d_in[9]; const u16* gw=(const u16*)d_in[10];
  float* ws=(float*)d_ws; float* q=ws; float* hb=q+B*H*D; float* qa=hb+B*F; float* s=qa+B*H*DC; float* cc=s+(size_t)B*H*SK; float* ctx=cc+B*H*DC;
  unsigned long long* st=(unsigned long long*)((char*)d_ws+(128u<<20));
  kjump<<<256,64,0,stream>>>(st);
  kamp<<<1,64,0,stream>>>(st);
  k1<<<(H*D+F)/64,64,0,stream>>>(x,wq,wg,wu,q,hb);
  k2<<<H*DC/64,64,0,stream>>>(q,wuk,qa);
  k3<<<dim3(SK/64,H,B),64,0,stream>>>(qa,ckv,s);
  k4<<<dim3(H,B),512,0,stream>>>(s,ckv,cc);
  k5<<<H*D/64,64,0,stream>>>(cc,wuv,ctx);
  k6<<<dim3(M,B),256,0,stream>>>(ctx,hb,wo,wd,x,gw,(u16*)d_out);
}
```

```cpp
#include <hip/hip_runtime.h>
#include <stdint.h>

typedef unsigned short u16;
typedef unsigned long long u64;
typedef unsigned v4u __attribute__((ext_vector_type(4)));
typedef unsigned v2u __attribute__((ext_vector_type(2)));
typedef float f32x4 __attribute__((ext_vector_type(4)));
typedef float f32x2 __attribute__((ext_vector_type(2)));
typedef short s16x4 __attribute__((ext_vector_type(4)));
typedef short s16x8 __attribute__((ext_vector_type(8)));
typedef __bf16 bf16x8 __attribute__((ext_vector_type(8)));
typedef __bf16 bf16x2 __attribute__((ext_vector_type(2)));
#define LAS __attribute__((address_space(3)))
#define DI __device__ __forceinline__

#define K_PRE 0
#define K_WIN 6
#define K_UKSPLIT 0
#define K_CSLEEP 16
#define K_UKSLEEP 5
#define K_H1SLEEP 16
#define K_GA 21
#define K_HINTN 4
#define K_HINTSLEEP 6
#define K_QSLEEP 56
#define K_ASLEEP 0
#define K_LASTSPLIT 0
#define K_STATFIRST 1
#define K_CODEPF 1
constexpr unsigned SPIN_CAP = 1u << 17;

constexpr unsigned W_E = 0, W_H1 = 1024, W_DONE = 16384;
constexpr unsigned W_QA = 65536;
constexpr unsigned W_CTX = W_QA + 131072;
constexpr unsigned W_HP = W_CTX + 262144;
constexpr unsigned W_OUTP = W_HP + 262144;
constexpr unsigned W_QP = W_OUTP + 262144;
constexpr unsigned W_REC = W_QP + 262144;
constexpr unsigned W_INBOX = 2097152;
constexpr unsigned W_STAMP = 8u << 20;

constexpr unsigned CROW = 1040, CTILE = 32 * CROW;
constexpr unsigned L_SB = 128 * CROW;
constexpr unsigned L_PB = L_SB + 4 * 32 * 144;
constexpr unsigned L_AL = L_PB + 32 * 80;
constexpr unsigned L_ML = L_AL + 128;
constexpr unsigned L_FL = L_ML + 256;
constexpr unsigned L_ACT = L_FL + 16;
constexpr unsigned L_GATE = L_ACT + 1600;
constexpr unsigned L_KEY = L_GATE + 32;
constexpr unsigned L_SCR = L_GATE + 64;
constexpr unsigned L_X = (L_SCR + 1024 + 255) & ~255u;
constexpr unsigned L_END = L_X + 1024;
static_assert(L_END <= 163840 && (L_SCR % 16) == 0 && (L_ACT % 16) == 0, "LDS");

struct Args {
  const u16 *wq, *x; unsigned char* ws; const u16 *ckv, *wuk, *wuv, *wo, *wg, *wu, *wd, *gw; u16* out;
};

#define DMA16(voff, sbase, m0v, IMM) \
  asm volatile("s_mov_b32 m0, %2\n\ts_nop 0\n\tglobal_load_lds_dwordx4 %0, %1 offset:%3" ::"v"(voff), "s"(sbase), "s"(m0v), "i"(IMM) : "memory")
#define GLD4(dst, voff, sbase, IMM, FL) \
  asm volatile("global_load_dwordx4 %0, %1, %2 offset:%3" FL : "=v"(dst) : "v"(voff), "s"(sbase), "i"(IMM) : "memory")
#define GLD2(dst, voff, sbase, IMM, FL) \
  asm volatile("global_load_dwordx2 %0, %1, %2 offset:%3" FL : "=v"(dst) : "v"(voff), "s"(sbase), "i"(IMM) : "memory")
#define GLD1(dst, voff, sbase, IMM, FL) \
  asm volatile("global_load_dword %0, %1, %2 offset:%3" FL : "=v"(dst) : "v"(voff), "s"(sbase), "i"(IMM) : "memory")
#define GST4(voff, src, sbase, IMM, FL) \
  asm volatile("global_store_dwordx4 %0, %1, %2 offset:%3" FL "\n\ts_nop 1" ::"v"(voff), "v"(src), "s"(sbase), "i"(IMM) : "memory")
#define GST4M(voff, src, sbase, IMM, FL)   \
  asm volatile("s_nop 7\n\tglobal_store_dwordx4 %0, %1, %2 offset:%3" FL "\n\ts_nop 1" ::"v"(voff), "v"(src), "s"(sbase), "i"(IMM) : "memory")
#define GST2(voff, src, sbase, IMM, FL) \
  asm volatile("global_store_dwordx2 %0, %1, %2 offset:%3" FL "\n\ts_nop 0" ::"v"(voff), "v"(src), "s"(sbase), "i"(IMM) : "memory")
#define GST1(voff, src, sbase, IMM, FL) \
  asm volatile("global_store_dword %0, %1, %2 offset:%3" FL ::"v"(voff), "v"(src), "s"(sbase), "i"(IMM) : "memory")
#define GSTH(voff, src, sbase, IMM, FL) \
  asm volatile("global_store_short %0, %1, %2 offset:%3" FL ::"v"(voff), "v"(src), "s"(sbase), "i"(IMM) : "memory")
#define SC1 " sc1"
#define NOF ""
#define VMW(N) asm volatile("s_waitcnt vmcnt(%0)" ::"i"(N) : "memory")
#define VMW1(N, a) asm volatile("s_waitcnt vmcnt(%1)" : "+v"(a) : "i"(N) : "memory")
#define VMW2(N, a, b) asm volatile("s_waitcnt vmcnt(%2)" : "+v"(a), "+v"(b) : "i"(N) : "memory")
#define VMW4(N, a, b, c, d) asm volatile("s_waitcnt vmcnt(%4)" : "+v"(a), "+v"(b), "+v"(c), "+v"(d) : "i"(N) : "memory")
#define BAR() asm volatile("s_waitcnt lgkmcnt(0)\n\ts_barrier" ::: "memory")

#define CST(i) do {} while (0)
#define STAMP(i) do {} while (0)
#define STAMPV(i, v) do {} while (0)

DI f32x4 mfma16(v4u a, v4u b, f32x4 c) {
  return __builtin_amdgcn_mfma_f32_16x16x32_bf16(__builtin_bit_cast(bf16x8, a), __builtin_bit_cast(bf16x8, b), c, 0, 0, 0);
}
DI unsigned pk2(float lo, float hi) { f32x2 t = {lo, hi}; return __builtin_bit_cast(unsigned, __builtin_convertvector(t, bf16x2)); }
DI float rbf(float v) { return __uint_as_float(pk2(v, v) << 16); }
DI float blo(unsigned u) { return __uint_as_float(u << 16); }
DI float bhi(unsigned u) { return __uint_as_float(u & 0xffff0000u); }
template <int CTRL> DI float dppf(float x) { return __builtin_bit_cast(float, __builtin_amdgcn_mov_dpp(__builtin_bit_cast(int, x), CTRL, 0xf, 0xf, true)); }
DI float rowmax16(float x) {
  float r;
  asm("s_nop 1\n\tv_max_f32_dpp %0, %1, %1 row_ror:8 row_mask:0xf bank_mask:0xf\n\ts_nop 1\n\t"
      "v_max_f32_dpp %0, %0, %0 row_ror:4 row_mask:0xf bank_mask:0xf\n\ts_nop 1\n\t"
      "v_max_f32_dpp %0, %0, %0 quad_perm:[2,3,0,1] row_mask:0xf bank_mask:0xf\n\ts_nop 1\n\t"
      "v_max_f32_dpp %0, %0, %0 quad_perm:[1,0,3,2] row_mask:0xf bank_mask:0xf\n\ts_nop 1" : "=&v"(r) : "v"(x));
  return r;
}
DI float vmax(float a, float b) { float r; asm("v_max_f32 %0, %1, %2" : "=v"(r) : "v"(a), "v"(b)); return r; }
#define PIN(x) asm volatile("" : "+v"(x))
#define SB0() __builtin_amdgcn_sched_barrier(0)
DI float rowsum16(float x) { x += dppf<0x128>(x); x += dppf<0x124>(x); x += dppf<0x4E>(x); return x + dppf<0xB1>(x); }
DI float swap32add(float a, float b) { auto s = __builtin_amdgcn_permlane32_swap(__float_as_uint(a), __float_as_uint(b), false, false); return __uint_as_float(s[0]) + __uint_as_float(s[1]); }
DI float swap16add(float a, float b) { auto s = __builtin_amdgcn_permlane16_swap(__float_as_uint(a), __float_as_uint(b), false, false); return __uint_as_float(s[0]) + __uint_as_float(s[1]); }
DI float swap32max(float a) { auto s = __builtin_amdgcn_permlane32_swap(__float_as_uint(a), __float_as_uint(a), false, false); return fmaxf(__uint_as_float(s[0]), __uint_as_float(s[1])); }
DI float swap16max(float a) { auto s = __builtin_amdgcn_permlane16_swap(__float_as_uint(a), __float_as_uint(a), false, false); return fmaxf(__uint_as_float(s[0]), __uint_as_float(s[1])); }
DI float tred4(float a0, float a1, float a2, float a3) { return swap16add(swap32add(a0, a2), swap32add(a1, a3)); }
DI unsigned umax4(v4u v) { return max(max(v.x, v.y), max(v.z, v.w)); }
struct Tag { unsigned t0, t1, t2, t3; };
DI Tag mktag(unsigned k0, unsigned k1) { return Tag{k0 & 0xfffu, (k0 >> 12) & 0xfffu, ((k0 >> 24) | (k1 << 8)) & 0xfffu, (k1 >> 4) & 0xfffu}; }
DI v4u settag(f32x4 v, Tag t) {
  return v4u{(__float_as_uint(v[0]) & ~0xfffu) | t.t0, (__float_as_uint(v[1]) & ~0xfffu) | t.t1, (__float_as_uint(v[2]) & ~0xfffu) | t.t2, (__float_as_uint(v[3]) & ~0xfffu) | t.t3};
}
DI unsigned badtag(v4u v, Tag t) { return (((v.x ^ t.t0) | (v.y ^ t.t1)) | ((v.z ^ t.t2) | (v.w ^ t.t3))) & 0xfffu; }
DI f32x4 untag(v4u v) { return f32x4{__uint_as_float(v.x & ~0xfffu), __uint_as_float(v.y & ~0xfffu), __uint_as_float(v.z & ~0xfffu), __uint_as_float(v.w & ~0xfffu)}; }
template <int CTRL> DI unsigned dppu(unsigned x) { return (unsigned)__builtin_amdgcn_mov_dpp((int)x, CTRL, 0xf, 0xf, true); }
DI unsigned fold4(v4u v) { return (v.x ^ v.y) ^ (v.z ^ v.w); }
DI unsigned rot16(unsigned x) { return (x << 16) | (x >> 16); }
DI bool allok(bool c) { return __builtin_amdgcn_ballot_w64(!c) == 0ull; }

DI v4u ldsr128(LAS unsigned char* L, unsigned off) { return *(LAS v4u*)(L + off); }
DI v4u ldstr(LAS unsigned char* L, unsigned off0, unsigned off1) {
  s16x4 a = __builtin_amdgcn_ds_read_tr16_b64_v4i16((LAS s16x4*)(L + off0));
  s16x4 b = __builtin_amdgcn_ds_read_tr16_b64_v4i16((LAS s16x4*)(L + off1));
  return __builtin_bit_cast(v4u, __builtin_shufflevector(a, b, 0, 1, 2, 3, 4, 5, 6, 7));
}

DI unsigned xw_base(unsigned img, int cw, int lane) {
  const int g = lane >> 4, q = (lane & 15) >> 2, p = lane & 3;
  return img + (8 * g + q) * 128 + (((2 * cw + (p >> 1)) ^ (2 * ((q >> 1) | ((g & 1) << 1)))) * 16) + 8 * (p & 1);
}
DI f32x4 xw128(LAS unsigned char* L, unsigned base, unsigned xb) {
  v4u bf[4], xf[4];
#pragma unroll
  for (int s = 0; s < 4; ++s) { xf[s] = ldsr128(L, xb ^ (s * 64)); bf[s] = ldstr(L, base + s * 4096, base + s * 4096 + 512); }
  __builtin_amdgcn_sched_barrier(0);
  f32x4 acc = {0.f, 0.f, 0.f, 0.f};
#pragma unroll
  for (int s = 0; s < 4; ++s) acc = mfma16(xf[s], bf[s], acc);
  return acc;
}

__global__ void __launch_bounds__(512, 2) fused_decode(Args a) {
  __shared__ __attribute__((aligned(16))) unsigned char smem[163840];
  LAS unsigned char* L = (LAS unsigned char*)smem;
  STAMP(0);
  const int tid = threadIdx.x, lane = tid & 63, w = __builtin_amdgcn_readfirstlane(tid >> 6);
  const int bid = blockIdx.x, xc = bid & 7, ii = (bid >> 3) & 15, isA = bid >> 7;
  const int h = 4 * xc + (ii >> 2), qq = ii & 3;
  const int g = lane >> 4, n16 = lane & 15;
  unsigned char* const ws = a.ws;

  v2u ev;
  if (w == 7) {
    const unsigned z = 0;
    GLD2(ev, z, ws, W_E, SC1);
    if (!isA) {
      const unsigned off = W_QA + ((g * 2 + (h >> 4)) * 4 + qq) * 4096 + n16 * 256 + (h & 15) * 16;
      const v4u sv = {~0u, ~0u, ~0u, ~0u};
      GST4(off, sv, ws, 0, SC1);
    }
  }
  {
    const unsigned row = 16 * w + (lane >> 3);
    const unsigned c0 = (lane & 7) ^ ((lane >> 3) & 2);
    const unsigned o0 = (128 * qq + row) * 8192 + h * 256 + isA * 128;
    const unsigned v0 = o0 + c0 * 16, v1 = o0 + 65536 + (c0 ^ 4) * 16;
    const unsigned m = L_SB + w * 2048;
    DMA16(v0, a.wq, m, 0);
    DMA16(v1, a.wq, m + 1024, 0);
  }
  if (w == 6) {
    u64 pc; unsigned len;
    asm volatile("s_getpc_b64 %0\n.Lpf_here:\n\ts_mov_b32 %1, .Lpf_end-.Lpf_here" : "=s"(pc), "=s"(len));
    const unsigned off = min((unsigned)((bid >> 3) * 1024 + lane * 16), len - 16u) & ~3u;
    DMA16(off, pc, L_SCR, 0);
  }
  if (w == 5) {
    const unsigned xo = g * 1024 + qq * 256 + ((n16 ^ (4 * g)) * 16);
    DMA16(xo, a.x, L_X, 0);
  }
  unsigned k0, k1;
  unsigned xb = L_X + (lane & 3) * 320 + g * 16, wqb = xw_base(L_SB, w & 3, lane);
  unsigned qpo = W_QP + ((h * 2 + isA) * 4 + qq) * 1024 + (16 * w + n16) * 16;
  Tag tg;
  if (w == 7) {
    VMW1(2, ev);
    const u64 e = ((u64)(unsigned)__builtin_amdgcn_readfirstlane(ev.y) << 32) | (unsigned)__builtin_amdgcn_readfirstlane(ev.x);
    const u64 k = e * 0x9E3779B97F4A7C15ull + 0x632BE59BD9B4E019ull;
    const v2u kv = {(unsigned)k, (unsigned)(k >> 32)};
    *(LAS v2u*)(L + L_KEY) = kv;
    *(LAS v4u*)(L + L_FL) = v4u{0u, 0u, 0u, 0u};
    STAMP(11);
    if (!isA) {
      const unsigned off1 = W_H1 + (qq * 32 + h) * 8;
      if (lane == 0) GST2(off1, kv, ws, 0, SC1);
    }
  }
#define KEY_GET() do { const v2u kv_ = *(LAS v2u*)(L + L_KEY); k0 = __builtin_amdgcn_readfirstlane(kv_.x); k1 = __builtin_amdgcn_readfirstlane(kv_.y); tg = mktag(k0, k1); } while (0)
#define Q_PHASE() do { KEY_GET(); if (w < 4) { const v4u gv_ = settag(xw128(L, wqb, xb), tg); if (lane < 16) GST4(qpo, gv_, ws, 0, SC1); } } while (0)

  if (!isA) {
#define UKDMA() do { const unsigned o_ = (128 * 2 * (w & 1) + 8 * (4 * qq + (w >> 1)) + g) * 8192 + h * 256; const unsigned m_ = w * 4096; \
      DMA16(o_ + ((n16 ^ g) * 16), a.wuk, m_, 0); DMA16(o_ + 4 * 8192 + ((n16 ^ (4 + g)) * 16), a.wuk, m_ + 1024, 0); \
      DMA16(o_ + 128 * 8192 + ((n16 ^ (8 + g)) * 16), a.wuk, m_ + 2048, 0); DMA16(o_ + 132 * 8192 + ((n16 ^ (12 + g)) * 16), a.wuk, m_ + 3072, 0); } while (0)
    if (w < 4 || !K_UKSPLIT) { UKDMA(); if (w == 7) VMW(5); else VMW(4); } else if (w == 7) VMW(1); else VMW(0);
    PIN(xb); PIN(wqb); PIN(qpo);
    BAR();
    STAMP(1);
    Q_PHASE();
    if (K_UKSPLIT && w >= 4) { if (K_UKSLEEP) __builtin_amdgcn_s_sleep(K_UKSLEEP); UKDMA(); }
    v4u uk[4];
    {
      unsigned ub = (16 * w + n16) * 256 + ((g ^ n16) * 16);
      PIN(ub);
      if (w < 4) VMW(1); else VMW(0);
#pragma unroll
      for (int s = 0; s < 4; ++s) uk[s] = ldsr128(L, ub ^ (s * 64));
    }
    STAMP(2);
    unsigned qao = W_QA + (((lane & 3) * 2 + (h >> 4)) * 4 + qq) * 4096 + (w >> 1) * 1024 + (2 * (w & 1) + (g >> 1)) * 256 + (h & 15) * 16;
    unsigned actw = L_ACT + g * 400 + (16 * w + n16) * 2, actr = L_ACT + (lane & 3) * 400 + g * 16;
    {
      const unsigned off = W_QP + ((h * 2 + (w >> 2)) * 4 + g) * 1024 + (16 * (w & 3) + n16) * 16;
      PIN(qao); PIN(actw); PIN(actr);
      v4u g0;
      unsigned spins = 0;
      for (;;) {
        GLD4(g0, off, ws, 0, SC1);
        VMW1(0, g0);
        if (allok(badtag(g0, tg) == 0u) || ++spins > SPIN_CAP) break;
      }
      STAMPV(12, spins);
      const f32x4 qp = untag(g0);
      const float qv = tred4(qp[0], qp[1], qp[2], qp[3]);
      *(LAS u16*)(L + actw) = (u16)pk2(qv, qv);
    }
    BAR();
    STAMP(3);
    {
      v4u qf[4];
#pragma unroll
      for (int s = 0; s < 4; ++s) qf[s] = ldsr128(L, actr + s * 64);
      SB0();
      f32x4 acc = {0.f, 0.f, 0.f, 0.f};
#pragma unroll
      for (int s = 0; s < 4; ++s) acc = mfma16(uk[s], qf[s], acc);
      constexpr float SC = 0.08838834764831845f * 1.4426950408889634f;
      const unsigned d0 = pk2(acc[0] * SC, acc[1] * SC), d1 = pk2(acc[2] * SC, acc[3] * SC);
      const auto s0 = __builtin_amdgcn_permlane16_swap(d0, d0, false, false), s1 = __builtin_amdgcn_permlane16_swap(d1, d1, false, false);
      const v4u o = {s0[0], s1[0], s0[1], s1[1]};
      if ((lane & 0x1c) == 0) GST4(qao, o, ws, 0, SC1);
    }
    STAMP(4);
    BAR();
    if (K_QSLEEP) __builtin_amdgcn_s_sleep(K_QSLEEP);
    STAMP(5);
    {
      const unsigned o0 = (128 * qq + 16 * w + g) * 8192 + h * 256;
      const unsigned ca = (n16 ^ (2 * g)) * 16, cb = ca ^ 128;
      const unsigned m = w * 4096;
      DMA16(o0 + ca, a.wuv, m, 0);
      DMA16(o0 + 32768 + ca, a.wuv, m + 1024, 0);
      DMA16(o0 + 65536 + cb, a.wuv, m + 2048, 0);
      DMA16(o0 + 98304 + cb, a.wuv, m + 3072, 0);
      if (h < 28) {
        const u16* src = (w >> 2) ? a.wu : a.wg;
        const unsigned o1 = (128 * qq + 32 * (w & 3) + (lane >> 3)) * 3584 + h * 128;
        const unsigned c0 = ((lane & 7) ^ ((lane >> 3) & 2)) * 16, c1 = c0 ^ 64;
        const unsigned m1 = 32768 + w * 4096;
        DMA16(o1 + c0, src, m1, 0);
        DMA16(o1 + 8 * 3584 + c1, src, m1 + 1024, 0);
        DMA16(o1 + 16 * 3584 + c0, src, m1 + 2048, 0);
        DMA16(o1 + 24 * 3584 + c1, src, m1 + 3072, 0);
      }
    }
    VMW(0);
    BAR();
    STAMP(6);
    if (h < 28) {
      const v4u gu = settag(xw128(L, xw_base(32768 + (w >> 2) * 16384, w & 3, lane), xb), tg);
      const unsigned off = W_HP + ((h * 4 + qq) * 2 + (w >> 2)) * 1024 + (16 * (w & 3) + n16) * 16;
      if (lane < 16) GST4(off, gu, ws, 0, SC1);
    }
    v4u uv[4];
    {
      const int q = n16 >> 2, p = lane & 3;
      const unsigned base = (8 * g + q) * 256 + (((2 * w + (p >> 1)) ^ (2 * (q | ((g & 1) << 2)))) * 16) + 8 * (p & 1);
#pragma unroll
      for (int s = 0; s < 4; ++s) uv[s] = ldstr(L, base + s * 8192, base + s * 8192 + 1024);
    }
    STAMP(7);
    {
      const int b = w >> 1, ch = w & 1, g8 = lane >> 3;
      unsigned so = W_REC + ((h * 4 + qq) * 128 + 32 * b + (lane & 31)) * 16;
      unsigned io = W_INBOX + ((h * 4 + qq) * 128 + 32 * b + g8) * 256 + ch * 128 + (lane & 7) * 16 + 4096;
      PIN(so); PIN(io);
      unsigned spins = 0;
      if (w < K_HINTN) {
        const unsigned ho = W_DONE + ((bid + 37 * w) & 127) * 128;
        unsigned hv;
        for (;;) {
          GLD1(hv, ho, ws, 0, SC1);
          VMW1(0, hv);
          if (allok(hv == k0) || ++spins > SPIN_CAP) break;
          __builtin_amdgcn_s_sleep(1);
        }
      }
      BAR();
      if (K_HINTSLEEP) __builtin_amdgcn_s_sleep(K_HINTSLEEP);
      STAMP(8);
      v4u d0, d1, d2, d3;
      float wr[4];
      const unsigned kc = ch ? k1 : k0;
      spins = 0;
      for (;;) {
        v4u rc;
        GLD4(rc, so, ws, 0, SC1);
        GLD4(d0, io, ws, -4096, SC1); GLD4(d1, io, ws, -2048, SC1); GLD4(d2, io, ws, 0, SC1); GLD4(d3, io, ws, 2048, SC1);
        VMW1(4, rc);
        const float ms = __uint_as_float(rc.x), ls = __uint_as_float(rc.y);
        const unsigned ex = (ch ? rc.w : rc.z) ^ kc ^ rc.x ^ rot16(rc.y);
        const float mx = swap16max(rowmax16(ms));
        const float e = __builtin_amdgcn_exp2f(ms - mx);
        const float rs = rowsum16(e * ls);
        const float en = e * __builtin_amdgcn_rcpf(swap16add(rs, rs));
        unsigned xr[4];
#pragma unroll
        for (int r = 0; r < 4; ++r) {
          wr[r] = __builtin_bit_cast(float, __builtin_amdgcn_ds_bpermute((8 * r + g8) * 4, __builtin_bit_cast(int, en)));
          xr[r] = (unsigned)__builtin_amdgcn_ds_bpermute((8 * r + g8) * 4, (int)ex);
        }
        SB0();
        VMW4(0, d0, d1, d2, d3);
        unsigned f0 = fold4(d0), f1 = fold4(d1), f2 = fold4(d2), f3 = fold4(d3);
        f0 ^= dppu<0xB1>(f0); f1 ^= dppu<0xB1>(f1); f2 ^= dppu<0xB1>(f2); f3 ^= dppu<0xB1>(f3);
        f0 ^= dppu<0x4E>(f0); f1 ^= dppu<0x4E>(f1); f2 ^= dppu<0x4E>(f2); f3 ^= dppu<0x4E>(f3);
        f0 ^= dppu<0x141>(f0); f1 ^= dppu<0x141>(f1); f2 ^= dppu<0x141>(f2); f3 ^= dppu<0x141>(f3);
        if (allok((((f0 ^ xr[0]) | (f1 ^ xr[1])) | ((f2 ^ xr[2]) | (f3 ^ xr[3]))) == 0u) || ++spins > SPIN_CAP) break;
      }
      STAMPV(13, spins);
      STAMP(9);
      float c[8];
#pragma unroll
      for (int j = 0; j < 4; ++j) {
        c[2 * j] = fmaf(wr[3], blo(d3[j]), fmaf(wr[2], blo(d2[j]), fmaf(wr[1], blo(d1[j]), wr[0] * blo(d0[j]))));
        c[2 * j + 1] = fmaf(wr[3], bhi(d3[j]), fmaf(wr[2], bhi(d2[j]), fmaf(wr[1], bhi(d1[j]), wr[0] * bhi(d0[j]))));
      }
      const float b0 = swap32add(c[0], c[4]), b1 = swap32add(c[1], c[5]), b2 = swap32add(c[2], c[6]), b3 = swap32add(c[3], c[7]);
      const float e0 = swap16add(b0, b2), e1 = swap16add(b1, b3);
      const bool hi8 = (lane & 8) != 0;
      const float fin = (hi8 ? e1 : e0) + dppf<0x128>(hi8 ? e0 : e1);
      *(LAS u16*)(L + L_ACT + b * 400 + (16 * g8 + 8 * ch + (lane & 7)) * 2) = (u16)pk2(fin, fin);
    }
    BAR();
    {
      v4u af[4];
#pragma unroll
      for (int s = 0; s < 4; ++s) af[s] = ldsr128(L, actr + s * 64);
      SB0();
      f32x4 acc = {0.f, 0.f, 0.f, 0.f};
#pragma unroll
      for (int s = 0; s < 4; ++s) acc = mfma16(af[s], uv[s], acc);
      const unsigned off = W_CTX + (h * 4 + qq) * 2048 + (16 * w + n16) * 16;
      const v4u cv = settag(acc, tg);
      if (lane < 16) GST4(off, cv, ws, 0, SC1);
    }
    STAMP(10);
    if (qq == 0 && w == 0) {
      const unsigned off = W_OUTP + ((h >> 3) * 32 + g) * 2048 + (16 * (h & 7) + n16) * 16;
      v4u o[8];
      unsigned spins = 0;
      for (;;) {
#pragma unroll
        for (int i = 0; i < 8; ++i) { const unsigned oi = off + i * 8192; GLD4(o[i], oi, ws, 0, SC1); }
        VMW4(0, o[0], o[1], o[2], o[3]); VMW4(0, o[4], o[5], o[6], o[7]);
        unsigned m = 0;
#pragma unroll
        for (int i = 0; i < 8; ++i) m |= badtag(o[i], tg);
        if (allok(m == 0u) || ++spins > SPIN_CAP) break;
      }
      f32x4 s = untag(o[0]);
#pragma unroll
      for (int i = 1; i < 8; ++i) s += untag(o[i]);
      const float r = tred4(s[0], s[1], s[2], s[3]);
      const unsigned oo = (g * 512 + 128 * (h >> 3) + 16 * (h & 7) + n16) * 2;
      const unsigned rb = pk2(r, r);
      GSTH(oo, rb, a.out, 0, NOF);
      if (h == 0) {
        const v2u kv = {k0, k1};
        const unsigned z = 0;
        if (lane == 0) GST2(z, kv, ws, W_E, SC1);
      }
      STAMP(11);
    }
  } else {
    const int b = xc >> 1, p = 32 * b + (xc & 1) * 16 + ii;
    const int ht = w & 1, kq = w >> 1;
    const u16* cb = a.ckv + (size_t)p * 65536;
    const unsigned cv = lane * 16 + (w & 3) * 32768, cm = (w & 3) * 8 * CROW;
#define CDMA(n) DMA16(cv, cb + ((n) >> 2) * 2048, cm + ((n) >> 3) * CTILE + ((n) & 7) * CROW - ((n) & 3) * 1024, ((n) & 3) * 1024)
    VMW(0);
    PIN(xb); PIN(wqb); PIN(qpo);
    BAR();
    STAMP(1);
    Q_PHASE();
    const unsigned h1o = W_H1 + lane * 16;
#define H1OK(v) allok((((v.x ^ k0) | (v.y ^ k1)) | ((v.z ^ k0) | (v.w ^ k1))) == 0u)
    if (w >= 4) {
      if (K_CSLEEP) __builtin_amdgcn_s_sleep(K_CSLEEP);
#pragma unroll
      for (int n = K_PRE; n < K_GA; ++n) {
        if (n >= K_WIN) VMW(K_WIN - 1);
        CDMA(n);
      }
    } else if (w == 0) {
      __builtin_amdgcn_s_sleep(K_H1SLEEP);
      v4u h1;
      GLD4(h1, h1o, ws, 0, SC1);
      VMW1(0, h1);
      if (H1OK(h1)) *(LAS unsigned*)(L + L_FL) = 1u;
    }
    STAMP(2);
    unsigned qk_base = (8 * (n16 >> 3) + 2 * (n16 & 3) + ((n16 >> 2) & 1)) * CROW + g * 256 + kq * 64;
    unsigned pv_base = (8 * g + 2 * (n16 >> 2)) * CROW + kq * 256 + (lane & 3) * 8;
    unsigned qk_hi = qk_base + 2 * CTILE, pv_hi = pv_base + 2 * CTILE;
    unsigned sw_off = L_SB + kq * 4608 + (16 * ht + n16) * 144 + g * 16;
    unsigned sr_off = L_SB + (4 * w + g) * 144 + n16 * 8;
    unsigned pw_off = L_PB + (4 * w + g) * 80 + n16 * 4, pr_off = L_PB + (16 * ht + n16) * 80 + g * 16;
    unsigned aw_off = L_AL + (4 * w + g) * 4, ar_off = L_AL + (16 * ht + 4 * g) * 4;
    unsigned qao = W_QA + ((b * 2 + ht) * 4 + kq) * 4096 + lane * 16;
    PIN(qk_base); PIN(pv_base); PIN(qk_hi); PIN(pv_hi); PIN(sw_off); PIN(sr_off); PIN(pw_off); PIN(pr_off); PIN(aw_off); PIN(ar_off); PIN(qao);
#define RDQK(F, t) do { _Pragma("unroll") for (int s = 0; s < 4; ++s) { \
      F[2 * s] = ldsr128(L, ((t) < 2 ? qk_base : qk_hi) + ((t) & 1) * CTILE + s * 16); \
      F[2 * s + 1] = ldsr128(L, ((t) < 2 ? qk_base : qk_hi) + ((t) & 1) * CTILE + 16 * CROW + s * 16); } } while (0)
#define RDPV(F, t) do { _Pragma("unroll") for (int c = 0; c < 8; ++c) \
      F[c] = ldstr(L, ((t) < 2 ? pv_base : pv_hi) + ((t) & 1) * CTILE + c * 32, ((t) < 2 ? pv_base : pv_hi) + ((t) & 1) * CTILE + CROW + c * 32); } while (0)
    static_assert(K_GA >= 16 && K_GA <= 29, "tiles 0 and 1 are published in front of the first gather");
    VMW(K_GA - 16 < K_WIN - 1 ? K_GA - 16 : K_WIN - 1);
    BAR();
    STAMP(4);
    if (__builtin_amdgcn_readfirstlane(*(LAS unsigned*)(L + L_FL)) == 0u) {
      v4u h1;
      unsigned spins = 0;
      do { GLD4(h1, h1o, ws, 0, SC1); VMW1(0, h1); } while (!H1OK(h1) && ++spins <= SPIN_CAP);
    }
    v4u qa[4], fk0[8], fk1[8], fv0[8], fv1[8];
    {
#define QAG() do { GLD4(qa[0], qao, ws, 0, SC1); GLD4(qa[1], qao, ws, 1024, SC1); GLD4(qa[2], qao, ws, 2048, SC1); GLD4(qa[3], qao, ws, 3072, SC1); } while (0)
#define QAOK() allok(max(max(umax4(qa[0]), umax4(qa[1])), max(umax4(qa[2]), umax4(qa[3]))) != ~0u)
      constexpr int R = 32 - K_GA, D1 = K_WIN < R ? K_WIN : R, D2 = R - D1;
      unsigned tries = 1;
      QAG();
      if (w >= 4) {
#pragma unroll
        for (int n = 0; n < D1; ++n) CDMA(K_GA + n);
      }
      RDQK(fk0, 0); RDQK(fk1, 1); RDPV(fv0, 0); RDPV(fv1, 1);
      SB0();
      if (w < 4) VMW(0);
      VMW4(D1, qa[0], qa[1], qa[2], qa[3]);
      while (!QAOK() && ++tries <= SPIN_CAP) { QAG(); VMW4(0, qa[0], qa[1], qa[2], qa[3]); }
      if (w >= 4) {
#pragma unroll
        for (int n = 0; n < D2; ++n) CDMA(K_GA + D1 + n);
      }
      STAMPV(12, tries);
    }
    STAMP(3);
    f32x4 S0, S1, T0, T1, O[8];
    f32x2 sa0, sa1, sa2, sa3;
    v4u pf;
    unsigned fl;
    float mrun, lrun;
    const f32x4 Z4 = {0.f, 0.f, 0.f, 0.f};
#define QS(A, B, F, s) do { A = mfma16(F[2 * (s)], qa[s], (s) ? A : Z4); B = mfma16(F[2 * (s) + 1], qa[s], (s) ? B : Z4); SB0(); } while (0)
#define PS(F, t, c) do { O[c] = mfma16(pf, F[c], (t) ? O[c] : Z4); O[(c) + 1] = mfma16(pf, F[(c) + 1], (t) ? O[(c) + 1] : Z4); SB0(); } while (0)
#define SWR(A, B) do { *(LAS f32x4*)(L + sw_off) = A; *(LAS f32x4*)(L + sw_off + 64) = B; } while (0)
    unsigned sr1 = sr_off + 4608, sr2 = sr_off + 9216, sr3 = sr_off + 13824;
    PIN(sr1); PIN(sr2); PIN(sr3);
#define SRD() do { sa0 = *(LAS f32x2*)(L + sr_off); sa1 = *(LAS f32x2*)(L + sr1); sa2 = *(LAS f32x2*)(L + sr2); sa3 = *(LAS f32x2*)(L + sr3); } while (0)
#define SM(t) do { \
      const f32x2 sv_ = (sa0 + sa1) + (sa2 + sa3); \
      if ((t) == 0) { mrun = rowmax16(vmax(sv_[0], sv_[1])); lrun = 0.f; } \
      else if (__builtin_amdgcn_ballot_w64(!(vmax(sv_[0], sv_[1]) - mrun <= 16.f)) != 0ull) { \
        const float mn_ = vmax(mrun, rowmax16(vmax(sv_[0], sv_[1]))), al_ = __builtin_amdgcn_exp2f(mrun - mn_); \
        lrun *= al_; mrun = mn_; *(LAS float*)(L + aw_off) = al_; \
        if (lane == 0) __hip_atomic_fetch_or((LAS unsigned*)(L + L_FL + 4 * (t)), 1u << w, __ATOMIC_RELAXED, __HIP_MEMORY_SCOPE_WORKGROUP); } \
      const float p0_ = __builtin_amdgcn_exp2f(sv_[0] - mrun), p1_ = __builtin_amdgcn_exp2f(sv_[1] - mrun); \
      *(LAS unsigned*)(L + pw_off) = pk2(p0_, p1_); \
      lrun += p0_ + p1_; SB0(); } while (0)
#define PRD(t) do { pf = ldsr128(L, pr_off); if ((t) > 0) fl = *(LAS unsigned*)(L + L_FL + 4 * (t)); } while (0)
#define RESC() do { const unsigned f_ = __builtin_amdgcn_readfirstlane(fl); \
      if (f_) { f32x4 av_ = *(LAS f32x4*)(L + ar_off); if (!((f_ >> (4 * ht + g)) & 1u)) av_ = f32x4{1.f, 1.f, 1.f, 1.f}; \
        _Pragma("unroll") for (int c = 0; c < 8; ++c) O[c] *= av_; } SB0(); } while (0)
#define RK(F, t, i) F[i] = ldsr128(L, ((t) < 2 ? qk_base : qk_hi) + ((t) & 1) * CTILE + ((i) & 1) * 16 * CROW + ((i) >> 1) * 16)
#define RV(F, t, c) F[c] = ldstr(L, ((t) < 2 ? pv_base : pv_hi) + ((t) & 1) * CTILE + (c) * 32, ((t) < 2 ? pv_base : pv_hi) + ((t) & 1) * CTILE + CROW + (c) * 32)
#define NOP_(F, t, i) do {} while (0)
#define QSX(A, B, F, s, RD, G, t) do { RD(G, t, 2 * (s)); RD(G, t, 2 * (s) + 1); SB0(); QS(A, B, F, s); } while (0)
#define PSX(F, t, c, RD, G, u) do { RD(G, u, c); RD(G, u, (c) + 1); SB0(); PS(F, t, c); } while (0)
#define QK4(A, B, F, RD, G, t) do { QSX(A, B, F, 0, RD, G, t); QSX(A, B, F, 1, RD, G, t); QSX(A, B, F, 2, RD, G, t); QSX(A, B, F, 3, RD, G, t); } while (0)
#define QKSM(A, B, F, u, RD, G, t) do { QSX(A, B, F, 0, RD, G, t); QSX(A, B, F, 1, RD, G, t); SM(u); QSX(A, B, F, 2, RD, G, t); QSX(A, B, F, 3, RD, G, t); } while (0)
#define PVSM(F, t, u, RD, G, v) do { PSX(F, t, 0, RD, G, v); PSX(F, t, 2, RD, G, v); SM(u); PSX(F, t, 4, RD, G, v); PSX(F, t, 6, RD, G, v); } while (0)
#define PV8(F, t) do { PS(F, t, 0); PS(F, t, 2); PS(F, t, 4); PS(F, t, 6); } while (0)
    CST(0);
    QK4(S0, S1, fk0, NOP_, fk0, 0); SWR(S0, S1); VMW(8); BAR(); CST(1);
    SRD(); SB0(); QKSM(T0, T1, fk1, 0, RK, fk0, 2); BAR(); CST(2);
    SWR(T0, T1); PRD(0); SB0(); QK4(S0, S1, fk0, RV, fk1, 2); VMW(0); BAR(); CST(3);
    STAMP(5);
    SRD(); SB0(); PVSM(fv0, 0, 1, RK, fk0, 3); BAR(); CST(4);
    SWR(S0, S1); PRD(1); SB0(); QK4(T0, T1, fk0, RV, fv0, 3); RESC(); BAR(); CST(5);
    SRD(); SB0(); PVSM(fv1, 1, 2, NOP_, fk0, 0); BAR(); CST(6);
    SWR(T0, T1); PRD(2); SB0(); RESC(); BAR(); CST(7);
    SRD(); SB0(); PVSM(fk1, 2, 3, NOP_, fk0, 0);
    { const f32x2 ml = {mrun, rowsum16(lrun)}; *(LAS f32x2*)(L + L_ML + (4 * w + g) * 8) = ml; }
    BAR(); CST(8);
    STAMP(6);
    PRD(3); SB0(); RESC(); PV8(fv0, 3); CST(9);
    {
      const unsigned off = W_INBOX + (((16 * ht + 4 * g) * 4 + kq) * 128 + p) * 256 + n16 * 16;
      const v2u ml = *(LAS v2u*)(L + L_ML + (16 * ht + 4 * g + (lane & 3)) * 8);
      unsigned y[4];
#pragma unroll
      for (int r = 0; r < 4; ++r) {
        const v4u o = {pk2(O[0][r], O[1][r]), pk2(O[2][r], O[3][r]), pk2(O[4][r], O[5][r]), pk2(O[6][r], O[7][r])};
        const unsigned of = off + r * 131072;
        GST4(of, o, ws, 0, SC1);
        y[r] = fold4(o);
      }
      const bool b0 = lane & 1, b1 = lane & 2;
      const unsigned t01 = (b0 ? y[1] : y[0]) ^ dppu<0xB1>(b0 ? y[0] : y[1]), t23 = (b0 ? y[3] : y[2]) ^ dppu<0xB1>(b0 ? y[2] : y[3]);
      unsigned u = (b1 ? t23 : t01) ^ dppu<0x4E>(b1 ? t01 : t23);
      u ^= dppu<0x124>(u);
      const unsigned mix = ml.x ^ rot16(ml.y);
      const v4u rec = {ml.x, ml.y, u ^ mix ^ k0, dppu<0x128>(u) ^ mix ^ k1};
      const unsigned ro = W_REC + (((16 * ht + 4 * g + (lane & 3)) * 4 + kq) * 128 + p) * 16;
      if ((n16 >> 2) == 1) GST4(ro, rec, ws, 0, SC1);
      if (w == 7) {
        const v4u dv = {k0, k0, k0, k0};
        const unsigned dn = W_DONE + p * 128 + lane * 16;
        if (lane < 8) GST4(dn, dv, ws, 0, SC1);
      }
    }
    CST(10);
    BAR();
    CST(11);
    STAMP(7);
    if (K_ASLEEP) __builtin_amdgcn_s_sleep(K_ASLEEP);
    {
      const unsigned ca = (n16 ^ (2 * g)) * 16, cc = ca ^ 128;
      const unsigned o0 = (h * 128 + 16 * w + g) * 1024 + qq * 256;
      const unsigned m = w * 4096;
      DMA16(o0 + ca, a.wo, m, 0);
      DMA16(o0 + 4096 + ca, a.wo, m + 1024, 0);
      DMA16(o0 + 8192 + cc, a.wo, m + 2048, 0);
      DMA16(o0 + 12288 + cc, a.wo, m + 3072, 0);
      const unsigned cd = (w & 1) ? cc : ca;
      const unsigned r0 = min(8 * w + g, 55), r1 = min(8 * w + 4 + g, 55);
      DMA16((56 * h + r0) * 1024 + qq * 256 + cd, a.wd, 32768 + w * 2048, 0);
      DMA16((56 * h + r1) * 1024 + qq * 256 + cd, a.wd, 32768 + w * 2048 + 1024, 0);
    }
    if (w < 4) {
      v4u xv, gv;
      const unsigned xo = w * 1024 + lane * 16, go = lane * 16;
      GLD4(xv, xo, a.x, 0, NOF); GLD4(gv, go, a.gw, 0, NOF);
      const unsigned fl = 16 * w + n16, f = 56 * h + min(fl, 55u);
      const unsigned ho = W_HP + ((f >> 6) * 4 + g) * 2048 + (f & 63) * 16;
      v4u g0, u0;
      GLD4(g0, ho, ws, 0, SC1); GLD4(u0, ho, ws, 1024, SC1);
      VMW4(0, xv, gv, g0, u0);
      float s = 0.f;
#pragma unroll
      for (int j = 0; j < 4; ++j) s += rbf(blo(xv[j]) * blo(gv[j])) + rbf(bhi(xv[j]) * bhi(gv[j]));
      s = rowsum16(s); s = swap16add(s, s); s = swap32add(s, s);
      const float gt = rbf(__builtin_amdgcn_rcpf(1.f + __expf(-rbf(s))));
      if (lane == 0) { *(LAS float*)(L + L_GATE + w * 4) = gt; *(LAS float*)(L + L_GATE + 16 + w * 4) = rbf(1.f - gt); }
      unsigned spins = 0;
      while (!allok((badtag(g0, tg) | badtag(u0, tg)) == 0u) && ++spins <= SPIN_CAP) { GLD4(g0, ho, ws, 0, SC1); GLD4(u0, ho, ws, 1024, SC1); VMW2(0, g0, u0); }
      const f32x4 gf = untag(g0), uf = untag(u0);
      const float gg = rbf(tred4(gf[0], gf[1], gf[2], gf[3]));
      const float uu = rbf(tred4(uf[0], uf[1], uf[2], uf[3]));
      const float hv = rbf(gg * __builtin_amdgcn_rcpf(1.f + __expf(-gg))) * uu;
      *(LAS u16*)(L + L_ACT + g * 400 + 256 + fl * 2) = fl < 56 ? (u16)pk2(hv, hv) : (u16)0;
    }
    VMW(0);
    BAR();
    STAMP(8);
    v4u wo[4];
    f32x4 accd = {0.f, 0.f, 0.f, 0.f};
    {
      const int q = n16 >> 2, pp = lane & 3;
      const unsigned base = (8 * g + q) * 256 + (((2 * w + (pp >> 1)) ^ (2 * (q | ((g & 1) << 2)))) * 16) + 8 * (pp & 1);
#pragma unroll
      for (int s = 0; s < 4; ++s) wo[s] = ldstr(L, base + s * 8192, base + s * 8192 + 1024);
#pragma unroll
      for (int s = 0; s < 2; ++s)
        accd = mfma16(ldsr128(L, L_ACT + (lane & 3) * 400 + 256 + g * 16 + s * 64), ldstr(L, base + 32768 + s * 8192, base + 32768 + s * 8192 + 1024), accd);
      accd *= *(LAS f32x4*)(L + L_GATE + 16);
    }
    const f32x4 gate = *(LAS f32x4*)(L + L_GATE);
    {
      const unsigned off = W_CTX + (h * 4 + g) * 2048 + (16 * w + n16) * 16;
      v4u c0;
      unsigned spins = 0;
      for (;;) {
        GLD4(c0, off, ws, 0, SC1);
        VMW1(0, c0);
        if (allok(badtag(c0, tg) == 0u) || ++spins > SPIN_CAP) break;
      }
      STAMP(9);
      const f32x4 cf = untag(c0);
      const float cvv = tred4(cf[0], cf[1], cf[2], cf[3]);
      *(LAS u16*)(L + L_ACT + g * 400 + (16 * w + n16) * 2) = (u16)pk2(cvv, cvv);
    }
    BAR();
    {
      v4u af[4];
#pragma unroll
      for (int s = 0; s < 4; ++s) af[s] = ldsr128(L, L_ACT + (lane & 3) * 400 + g * 16 + s * 64);
      SB0();
      f32x4 acc = {0.f, 0.f, 0.f, 0.f};
#pragma unroll
      for (int s = 0; s < 4; ++s) acc = mfma16(af[s], wo[s], acc);
      const v4u ov = settag(acc * gate + accd, tg);
      const unsigned off = W_OUTP + (qq * 32 + h) * 2048 + (16 * w + n16) * 16;
      if (lane < 16) GST4(off, ov, ws, 0, SC1);
    }
    STAMP(10);
  }
  asm volatile(".Lpf_end:");
}

extern "C" void kernel_launch(void* const* d_in, const int* in_sizes, int n_in, void* d_out, int out_size, void* d_ws, size_t ws_size,
                              hipStream_t stream) {
  Args a;
  a.x = (const u16*)d_in[0]; a.ckv = (const u16*)d_in[1]; a.wq = (const u16*)d_in[2]; a.wuk = (const u16*)d_in[3];
  a.wuv = (const u16*)d_in[4]; a.wo = (const u16*)d_in[5]; a.wg = (const u16*)d_in[7]; a.wu = (const u16*)d_in[8];
  a.wd = (const u16*)d_in[9]; a.gw = (const u16*)d_in[10]; a.out = (u16*)d_out; a.ws = (unsigned char*)d_ws;
  hipLaunchKernelGGL(fused_decode, dim3(256), dim3(512), 0, stream, a);
}
```

```cpp
#include <hip/hip_runtime.h>
#include <stdint.h>

typedef unsigned short u16;
typedef unsigned long long u64;
typedef unsigned v4u __attribute__((ext_vector_type(4)));
typedef unsigned v2u __attribute__((ext_vector_type(2)));
typedef float f32x4 __attribute__((ext_vector_type(4)));
typedef float f32x2 __attribute__((ext_vector_type(2)));
typedef float f32x16 __attribute__((ext_vector_type(16)));
typedef short s16x4 __attribute__((ext_vector_type(4)));
typedef short s16x8 __attribute__((ext_vector_type(8)));
typedef __bf16 bf16x8 __attribute__((ext_vector_type(8)));
typedef __bf16 bf16x2 __attribute__((ext_vector_type(2)));
#define LAS __attribute__((address_space(3)))
#define DI __device__ __forceinline__

#define K_PRE 4
#define K_WIN 8
#define K_QGSLEEP 17
#define K_CSLEEP 0
#define K_PWIN 4
#define K_HPOS 28
#define K_WOPF 1
#define K_GAFIRST 1
#define K_BACKOFF 10
#define K_D1 4
#define K_H1SLEEP 10
#define K_GA 23
#define K_HINTN 4
#define K_HINTSLEEP 5
#define K_QSLEEP 10
#define K_ASLEEP 0
#define K_WOEARLY 0
#define K_WARM 0
#define K_WARMSLEEP 14
#define K_CODEPF 1
constexpr unsigned SPIN_CAP = 1u << 17;

constexpr unsigned W_E = 0, W_H1 = 1024, W_DONE = 16384, W_NEAR = 32768;
constexpr unsigned W_QA = 65536;
constexpr unsigned W_CTX = W_QA + 131072;
constexpr unsigned W_HP = W_CTX + 262144;
constexpr unsigned W_OUTP = W_HP + 262144;
constexpr unsigned W_QP = W_OUTP + 262144;
constexpr unsigned W_REC = W_QP + 262144;
constexpr unsigned W_INBOX = 2097152;
constexpr unsigned W_STAMP = 8u << 20;

constexpr unsigned CROW = 1040, CTILE = 32 * CROW;
constexpr unsigned L_WQ = 3 * CTILE;
constexpr unsigned L_PB = 128 * CROW, PTILE = 32 * 80;
constexpr unsigned L_MX = L_PB + 4 * PTILE;
constexpr unsigned L_LS = L_MX + 512;
constexpr unsigned L_RC = L_LS + 512;
constexpr unsigned L_AL = L_RC + 1024;
constexpr unsigned L_M = L_AL + 128;
constexpr unsigned L_FL = L_M + 128;
constexpr unsigned L_ACT = L_FL + 16;
constexpr unsigned L_GATE = L_ACT + 1600;
constexpr unsigned L_KEY = L_GATE + 32;
constexpr unsigned L_SCR = L_PB;
constexpr unsigned L_X = (L_GATE + 64 + 255) & ~255u;
constexpr unsigned L_WD = L_X + 1024;
constexpr unsigned L_WO = CTILE;
constexpr unsigned L_END = L_WD + 56 * 256;
static_assert(L_END <= 163840 && (L_SCR % 16) == 0 && (L_ACT % 16) == 0, "LDS");

struct Args {
  const u16 *wq, *x; unsigned char* ws; const u16 *ckv, *wuk, *wuv, *wo, *wg, *wu, *wd, *gw; u16* out;
};

#define DMA16(voff, sbase, m0v, IMM) \
  asm volatile("s_mov_b32 m0, %2\n\ts_nop 0\n\tglobal_load_lds_dwordx4 %0, %1 offset:%3" ::"v"(voff), "s"(sbase), "s"(m0v), "i"(IMM) : "memory")
#define GLD4(dst, voff, sbase, IMM, FL) \
  asm volatile("global_load_dwordx4 %0, %1, %2 offset:%3" FL : "=v"(dst) : "v"(voff), "s"(sbase), "i"(IMM) : "memory")
#define GLD2(dst, voff, sbase, IMM, FL) \
  asm volatile("global_load_dwordx2 %0, %1, %2 offset:%3" FL : "=v"(dst) : "v"(voff), "s"(sbase), "i"(IMM) : "memory")
#define GLD1(dst, voff, sbase, IMM, FL) \
  asm volatile("global_load_dword %0, %1, %2 offset:%3" FL : "=v"(dst) : "v"(voff), "s"(sbase), "i"(IMM) : "memory")
#define GST4(voff, src, sbase, IMM, FL) \
  asm volatile("global_store_dwordx4 %0, %1, %2 offset:%3" FL "\n\ts_nop 1" ::"v"(voff), "v"(src), "s"(sbase), "i"(IMM) : "memory")
#define GST4M(voff, src, sbase, IMM, FL)   \
  asm volatile("s_nop 7\n\tglobal_store_dwordx4 %0, %1, %2 offset:%3" FL "\n\ts_nop 1" ::"v"(voff), "v"(src), "s"(sbase), "i"(IMM) : "memory")
#define GST2(voff, src, sbase, IMM, FL) \
  asm volatile("global_store_dwordx2 %0, %1, %2 offset:%3" FL "\n\ts_nop 0" ::"v"(voff), "v"(src), "s"(sbase), "i"(IMM) : "memory")
#define GST1(voff, src, sbase, IMM, FL) \
  asm volatile("global_store_dword %0, %1, %2 offset:%3" FL ::"v"(voff), "v"(src), "s"(sbase), "i"(IMM) : "memory")
#define GSTH(voff, src, sbase, IMM, FL) \
  asm volatile("global_store_short %0, %1, %2 offset:%3" FL ::"v"(voff), "v"(src), "s"(sbase), "i"(IMM) : "memory")
#define SC1 " sc1"
#define NOF ""
#define VMW(N) asm volatile("s_waitcnt vmcnt(%0)" ::"i"(N) : "memory")
#define VMW1(N, a) asm volatile("s_waitcnt vmcnt(%1)" : "+v"(a) : "i"(N) : "memory")
#define VMW2(N, a, b) asm volatile("s_waitcnt vmcnt(%2)" : "+v"(a), "+v"(b) : "i"(N) : "memory")
#define VMW4(N, a, b, c, d) asm volatile("s_waitcnt vmcnt(%4)" : "+v"(a), "+v"(b), "+v"(c), "+v"(d) : "i"(N) : "memory")
#define BAR() asm volatile("s_waitcnt lgkmcnt(0)\n\ts_barrier" ::: "memory")

#define SLEEP(K, i) do { if (K) __builtin_amdgcn_s_sleep(K); } while (0)
#define CST(i) do {} while (0)
#define STAMP(i) do {} while (0)
#define STAMPV(i, v) do {} while (0)

DI f32x4 mfma16(v4u a, v4u b, f32x4 c) {
  return __builtin_amdgcn_mfma_f32_16x16x32_bf16(__builtin_bit_cast(bf16x8, a), __builtin_bit_cast(bf16x8, b), c, 0, 0, 0);
}
DI unsigned pk2(float lo, float hi) { f32x2 t = {lo, hi}; return __builtin_bit_cast(unsigned, __builtin_convertvector(t, bf16x2)); }
DI float rbf(float v) { return __uint_as_float(pk2(v, v) << 16); }
DI float blo(unsigned u) { return __uint_as_float(u << 16); }
DI float bhi(unsigned u) { return __uint_as_float(u & 0xffff0000u); }
template <int CTRL> DI float dppf(float x) { return __builtin_bit_cast(float, __builtin_amdgcn_mov_dpp(__builtin_bit_cast(int, x), CTRL, 0xf, 0xf, true)); }
DI float rowmax16(float x) {
  float r;
  asm("s_nop 1\n\tv_max_f32_dpp %0, %1, %1 row_ror:8 row_mask:0xf bank_mask:0xf\n\ts_nop 1\n\t"
      "v_max_f32_dpp %0, %0, %0 row_ror:4 row_mask:0xf bank_mask:0xf\n\ts_nop 1\n\t"
      "v_max_f32_dpp %0, %0, %0 quad_perm:[2,3,0,1] row_mask:0xf bank_mask:0xf\n\ts_nop 1\n\t"
      "v_max_f32_dpp %0, %0, %0 quad_perm:[1,0,3,2] row_mask:0xf bank_mask:0xf\n\ts_nop 1" : "=&v"(r) : "v"(x));
  return r;
}
DI float vmax(float a, float b) { float r; asm("v_max_f32 %0, %1, %2" : "=v"(r) : "v"(a), "v"(b)); return r; }
DI float absmax3(float a, float b, float c) { float r; asm("v_max3_f32 %0, |%1|, |%2|, |%3|" : "=v"(r) : "v"(a), "v"(b), "v"(c)); return r; }
#define MF16Z(acc, a, b) asm volatile("v_mfma_f32_16x16x32_bf16 %0, %1, %2, 0" : "=&v"(acc) : "v"(a), "v"(b))
#define MF16(acc, a, b) asm volatile("v_mfma_f32_16x16x32_bf16 %0, %1, %2, %0" : "+v"(acc) : "v"(a), "v"(b))
#define MF32Z(acc, a, b) asm volatile("v_mfma_f32_32x32x16_bf16 %0, %1, %2, 0" : "=&v"(acc) : "v"(a), "v"(b))
#define MF32(acc, a, b) asm volatile("v_mfma_f32_32x32x16_bf16 %0, %1, %2, %0" : "+v"(acc) : "v"(a), "v"(b))
#define PIN(x) asm volatile("" : "+v"(x))
#define SB0() __builtin_amdgcn_sched_barrier(0)
DI float rowsum16(float x) { x += dppf<0x128>(x); x += dppf<0x124>(x); x += dppf<0x4E>(x); return x + dppf<0xB1>(x); }
DI float swap32add(float a, float b) { auto s = __builtin_amdgcn_permlane32_swap(__float_as_uint(a), __float_as_uint(b), false, false); return __uint_as_float(s[0]) + __uint_as_float(s[1]); }
DI float swap16add(float a, float b) { auto s = __builtin_amdgcn_permlane16_swap(__float_as_uint(a), __float_as_uint(b), false, false); return __uint_as_float(s[0]) + __uint_as_float(s[1]); }
DI float swap32max(float a) { auto s = __builtin_amdgcn_permlane32_swap(__float_as_uint(a), __float_as_uint(a), false, false); return fmaxf(__uint_as_float(s[0]), __uint_as_float(s[1])); }
DI float swap16max(float a) { auto s = __builtin_amdgcn_permlane16_swap(__float_as_uint(a), __float_as_uint(a), false, false); return fmaxf(__uint_as_float(s[0]), __uint_as_float(s[1])); }
DI float tred4(float a0, float a1, float a2, float a3) { return swap16add(swap32add(a0, a2), swap32add(a1, a3)); }
DI unsigned umax4(v4u v) { return max(max(v.x, v.y), max(v.z, v.w)); }
struct Tag { unsigned t0, t1, t2, t3; };
DI Tag mktag(unsigned k0, unsigned k1) { return Tag{k0 & 0xfffu, (k0 >> 12) & 0xfffu, ((k0 >> 24) | (k1 << 8)) & 0xfffu, (k1 >> 4) & 0xfffu}; }
DI v4u settag(f32x4 v, Tag t) {
  return v4u{(__float_as_uint(v[0]) & ~0xfffu) | t.t0, (__float_as_uint(v[1]) & ~0xfffu) | t.t1, (__float_as_uint(v[2]) & ~0xfffu) | t.t2, (__float_as_uint(v[3]) & ~0xfffu) | t.t3};
}
DI unsigned badtag(v4u v, Tag t) { return (((v.x ^ t.t0) | (v.y ^ t.t1)) | ((v.z ^ t.t2) | (v.w ^ t.t3))) & 0xfffu; }
DI f32x4 untag(v4u v) { return f32x4{__uint_as_float(v.x & ~0xfffu), __uint_as_float(v.y & ~0xfffu), __uint_as_float(v.z & ~0xfffu), __uint_as_float(v.w & ~0xfffu)}; }
template <int CTRL> DI unsigned dppu(unsigned x) { return (unsigned)__builtin_amdgcn_mov_dpp((int)x, CTRL, 0xf, 0xf, true); }
DI unsigned fold4(v4u v) { return (v.x ^ v.y) ^ (v.z ^ v.w); }
DI unsigned rot16(unsigned x) { return (x << 16) | (x >> 16); }
DI bool allok(bool c) { return __builtin_amdgcn_ballot_w64(!c) == 0ull; }

DI v4u ldsr128(LAS unsigned char* L, unsigned off) { return *(LAS v4u*)(L + off); }
DI v4u ldstr(LAS unsigned char* L, unsigned off0, unsigned off1) {
  s16x4 a = __builtin_amdgcn_ds_read_tr16_b64_v4i16((LAS s16x4*)(L + off0));
  s16x4 b = __builtin_amdgcn_ds_read_tr16_b64_v4i16((LAS s16x4*)(L + off1));
  return __builtin_bit_cast(v4u, __builtin_shufflevector(a, b, 0, 1, 2, 3, 4, 5, 6, 7));
}

DI unsigned xw_base(unsigned img, int cw, int lane) {
  const int g = lane >> 4, q = (lane & 15) >> 2, p = lane & 3;
  return img + (8 * g + q) * 128 + (((2 * cw + (p >> 1)) ^ (2 * ((q >> 1) | ((g & 1) << 1)))) * 16) + 8 * (p & 1);
}
DI f32x4 xw128(LAS unsigned char* L, unsigned base, unsigned xb) {
  v4u bf[4], xf[4];
#pragma unroll
  for (int s = 0; s < 4; ++s) { xf[s] = ldsr128(L, xb ^ (s * 64)); bf[s] = ldstr(L, base + s * 4096, base + s * 4096 + 512); }
  __builtin_amdgcn_sched_barrier(0);
  f32x4 acc = {0.f, 0.f, 0.f, 0.f};
#pragma unroll
  for (int s = 0; s < 4; ++s) acc = mfma16(xf[s], bf[s], acc);
  return acc;
}

__global__ void __launch_bounds__(512, 2) fused_decode(Args a) {
  __shared__ __attribute__((aligned(16))) unsigned char smem[163840];
  LAS unsigned char* L = (LAS unsigned char*)smem;
  STAMP(0);
  const int tid = threadIdx.x, lane = tid & 63, w = __builtin_amdgcn_readfirstlane(tid >> 6);
  const int bid = blockIdx.x, xc = bid & 7, ii = (bid >> 3) & 15, isA = bid >> 7;
  const int h = 4 * xc + (ii >> 2), qq = ii & 3;
  const int g = lane >> 4, n16 = lane & 15;
  unsigned char* const ws = a.ws;

  v2u ev;
  if (w == 7) {
    const unsigned z = 0;
    GLD2(ev, z, ws, W_E, SC1);
    if (!isA) {
      const unsigned off = W_QA + ((g * 2 + (h >> 4)) * 4 + qq) * 4096 + n16 * 256 + (h & 15) * 16;
      const v4u sv = {~0u, ~0u, ~0u, ~0u};
      GST4(off, sv, ws, 0, SC1);
    }
  }
  if (isA) {
    const unsigned o0 = (128 * qq + 16 * w + g) * 8192 + h * 256, ca = (n16 ^ (2 * g)) * 16, cc = ca ^ 128, m = L_WQ + w * 4096;
    DMA16(o0 + ca, a.wq, m, 0); DMA16(o0 + 32768 + ca, a.wq, m + 1024, 0); DMA16(o0 + 65536 + cc, a.wq, m + 2048, 0); DMA16(o0 + 98304 + cc, a.wq, m + 3072, 0);
  } else {
    const unsigned o0 = (256 * (w & 1) + 8 * (4 * qq + (w >> 1)) + g) * 8192 + h * 256, m = w * 4096;
    DMA16(o0 + ((n16 ^ g) * 16), a.wuk, m, 0); DMA16(o0 + 4 * 8192 + ((n16 ^ (4 + g)) * 16), a.wuk, m + 1024, 0);
    DMA16(o0 + 128 * 8192 + ((n16 ^ (8 + g)) * 16), a.wuk, m + 2048, 0); DMA16(o0 + 132 * 8192 + ((n16 ^ (12 + g)) * 16), a.wuk, m + 3072, 0);
  }
  if (w == 6) {
    u64 pc; unsigned len;
    asm volatile("s_getpc_b64 %0\n.Lpf_here:\n\ts_mov_b32 %1, .Lpf_end-.Lpf_here" : "=s"(pc), "=s"(len));
    const unsigned off = min((unsigned)((bid >> 3) * 1024 + lane * 16), len - 16u) & ~3u;
    DMA16(off, pc, L_SCR, 0);
  }
  if (w == 5) {
    const unsigned xo = g * 1024 + qq * 256 + ((n16 ^ (4 * g)) * 16);
    DMA16(xo, a.x, L_X, 0);
  }
  unsigned k0, k1;
  unsigned xb = L_X + (lane & 3) * 320 + g * 16;
  Tag tg;
  if (w == 7) {
    VMW1(4, ev);
    const u64 e = ((u64)(unsigned)__builtin_amdgcn_readfirstlane(ev.y) << 32) | (unsigned)__builtin_amdgcn_readfirstlane(ev.x);
    const u64 k = e * 0x9E3779B97F4A7C15ull + 0x632BE59BD9B4E019ull;
    const v2u kv = {(unsigned)k, (unsigned)(k >> 32)};
    *(LAS v2u*)(L + L_KEY) = kv;
    *(LAS unsigned*)(L + L_M + min(lane, 35) * 4) = 0u;
    if (!isA) {
      const unsigned off1 = W_H1 + (qq * 32 + h) * 8;
      if (lane == 0) GST2(off1, kv, ws, 0, SC1);
    }
  }
#define KEY_GET() do { const v2u kv_ = *(LAS v2u*)(L + L_KEY); k0 = __builtin_amdgcn_readfirstlane(kv_.x); k1 = __builtin_amdgcn_readfirstlane(kv_.y); tg = mktag(k0, k1); } while (0)

#define WODMA() do { const unsigned ca = (n16 ^ (2 * g)) * 16, cc = ca ^ 128; \
      const unsigned o0 = (h * 128 + 16 * w + g) * 1024 + qq * 256, m = L_WO + w * 4096; \
      DMA16(o0 + ca, a.wo, m, 0); DMA16(o0 + 4096 + ca, a.wo, m + 1024, 0); DMA16(o0 + 8192 + cc, a.wo, m + 2048, 0); DMA16(o0 + 12288 + cc, a.wo, m + 3072, 0); \
      const unsigned w6 = min(w, 6), o1 = (56 * h + 8 * w6 + g) * 1024 + qq * 256 + ((w6 & 1) ? cc : ca); \
      DMA16(o1, a.wd, L_WD + w6 * 2048, 0); DMA16(o1 + 4096, a.wd, L_WD + w6 * 2048 + 1024, 0); } while (0)

  if (!isA) {
    v4u uk[4];
    {
      unsigned ub = (16 * w + n16) * 256 + ((g ^ n16) * 16);
      PIN(ub);
      if (w != 7) VMW(0); else VMW(1);
      STAMP(1);
#pragma unroll
      for (int s = 0; s < 4; ++s) uk[s] = ldsr128(L, ub ^ (s * 64));
      BAR();
      KEY_GET();
      SLEEP(K_QGSLEEP, 0);
    }
    STAMP(2);
    unsigned qao = W_QA + (((lane & 3) * 2 + (h >> 4)) * 4 + qq) * 4096 + (w >> 1) * 1024 + (2 * (w & 1) + (g >> 1)) * 256 + (h & 15) * 16;
    unsigned actw = L_ACT + g * 400 + (16 * w + n16) * 2, actr = L_ACT + (lane & 3) * 400 + g * 16;
    {
      const unsigned off = W_QP + (h * 4 + g) * 2048 + (16 * w + n16) * 16;
      PIN(qao); PIN(actw); PIN(actr);
      v4u g0;
      unsigned spins = 0;
      for (;;) {
        GLD4(g0, off, ws, 0, SC1);
        VMW1(0, g0);
        if (allok(badtag(g0, tg) == 0u) || ++spins > SPIN_CAP) break;
        __builtin_amdgcn_s_sleep(4);
      }
      STAMPV(12, spins);
      const f32x4 qp = untag(g0);
      const float qv = tred4(qp[0], qp[1], qp[2], qp[3]);
      *(LAS u16*)(L + actw) = (u16)pk2(qv, qv);
    }
    BAR();
    STAMP(3);
    {
      v4u qf[4];
#pragma unroll
      for (int s = 0; s < 4; ++s) qf[s] = ldsr128(L, actr + s * 64);
      SB0();
      f32x4 acc = {0.f, 0.f, 0.f, 0.f};
#pragma unroll
      for (int s = 0; s < 4; ++s) acc = mfma16(uk[s], qf[s], acc);
      constexpr float SC = 0.08838834764831845f * 1.4426950408889634f;
      const unsigned d0 = pk2(acc[0] * SC, acc[1] * SC), d1 = pk2(acc[2] * SC, acc[3] * SC);
      const auto s0 = __builtin_amdgcn_permlane16_swap(d0, d0, false, false), s1 = __builtin_amdgcn_permlane16_swap(d1, d1, false, false);
      const v4u o = {s0[0], s1[0], s0[1], s1[1]};
      if ((lane & 0x1c) == 0) GST4(qao, o, ws, 0, SC1);
    }
    STAMP(4);
    if (w == 0) {
      SLEEP(K_QSLEEP, 3);
      const unsigned ho = W_NEAR + bid * 128;
      unsigned hv, spins = 0;
      for (;;) {
        GLD1(hv, ho, ws, 0, SC1);
        VMW1(0, hv);
        if (allok(hv == k0) || ++spins > SPIN_CAP) break;
        __builtin_amdgcn_s_sleep(2);
      }
    }
    BAR();
    STAMP(5);
    {
      const unsigned o0 = (128 * qq + 16 * w + g) * 8192 + h * 256;
      const unsigned ca = (n16 ^ (2 * g)) * 16, cb = ca ^ 128;
      const unsigned m = w * 4096;
      DMA16(o0 + ca, a.wuv, m, 0);
      DMA16(o0 + 32768 + ca, a.wuv, m + 1024, 0);
      DMA16(o0 + 65536 + cb, a.wuv, m + 2048, 0);
      DMA16(o0 + 98304 + cb, a.wuv, m + 3072, 0);
      if (h < 28) {
        const u16* src = (w >> 2) ? a.wu : a.wg;
        const unsigned o1 = (128 * qq + 32 * (w & 3) + (lane >> 3)) * 3584 + h * 128;
        const unsigned c0 = ((lane & 7) ^ ((lane >> 3) & 2)) * 16, c1 = c0 ^ 64;
        const unsigned m1 = 32768 + w * 4096;
        DMA16(o1 + c0, src, m1, 0);
        DMA16(o1 + 8 * 3584 + c1, src, m1 + 1024, 0);
        DMA16(o1 + 16 * 3584 + c0, src, m1 + 2048, 0);
        DMA16(o1 + 24 * 3584 + c1, src, m1 + 3072, 0);
      }
    }
    VMW(0);
    BAR();
    STAMP(6);
    if (h < 28) {
      const v4u gu = settag(xw128(L, xw_base(32768 + (w >> 2) * 16384, w & 3, lane), xb), tg);
      const unsigned off = W_HP + ((h * 4 + qq) * 2 + (w >> 2)) * 1024 + (16 * (w & 3) + n16) * 16;
      if (lane < 16) GST4(off, gu, ws, 0, SC1);
    }
    v4u uv[4];
    {
      const int q = n16 >> 2, p = lane & 3;
      const unsigned base = (8 * g + q) * 256 + (((2 * w + (p >> 1)) ^ (2 * (q | ((g & 1) << 2)))) * 16) + 8 * (p & 1);
#pragma unroll
      for (int s = 0; s < 4; ++s) uv[s] = ldstr(L, base + s * 8192, base + s * 8192 + 1024);
    }
    STAMP(7);
    {
      const int b = w >> 1, ch = w & 1, g8 = lane >> 3;
      unsigned so = W_REC + ((h * 128 + 32 * b + (lane & 31)) * 4 + qq) * 16;
      const unsigned kc = ch ? k1 : k0;
      unsigned io = W_INBOX + ((h * 4 + qq) * 128 + 32 * b + g8) * 256 + ch * 128 + (lane & 7) * 16 + 4096;
      PIN(so); PIN(io);
      unsigned spins = 0;
      if (w < K_HINTN) {
        const unsigned ho = W_DONE + ((bid + 37 * w) & 127) * 128;
        unsigned hv;
        for (;;) {
          GLD1(hv, ho, ws, 0, SC1);
          VMW1(0, hv);
          if (allok(hv == k0) || ++spins > SPIN_CAP) break;
          __builtin_amdgcn_s_sleep(1);
        }
      }
      BAR();
      SLEEP(K_HINTSLEEP, 4);
      STAMP(8);
      v4u d0, d1, d2, d3;
      float wr[4];
      spins = 0;
      for (;;) {
        v4u rc;
        GLD4(rc, so, ws, 0, SC1);
        GLD4(d0, io, ws, -4096, SC1); GLD4(d1, io, ws, -2048, SC1); GLD4(d2, io, ws, 0, SC1); GLD4(d3, io, ws, 2048, SC1);
        VMW1(4, rc);
        const float ms = __uint_as_float(rc.x), ls = __uint_as_float(rc.y);
        const unsigned ex = (ch ? rc.w : rc.z) ^ kc ^ rc.x ^ rot16(rc.y);
        const float mx = swap16max(rowmax16(ms));
        const float e = __builtin_amdgcn_exp2f(ms - mx);
        const float rs = rowsum16(e * ls);
        const float en = e * __builtin_amdgcn_rcpf(swap16add(rs, rs));
        unsigned xr[4];
#pragma unroll
        for (int r = 0; r < 4; ++r) {
          wr[r] = __builtin_bit_cast(float, __builtin_amdgcn_ds_bpermute((8 * r + g8) * 4, __builtin_bit_cast(int, en)));
          xr[r] = (unsigned)__builtin_amdgcn_ds_bpermute((8 * r + g8) * 4, (int)ex);
        }
        SB0();
        VMW4(0, d0, d1, d2, d3);
        unsigned f0 = fold4(d0), f1 = fold4(d1), f2 = fold4(d2), f3 = fold4(d3);
        f0 ^= dppu<0xB1>(f0); f1 ^= dppu<0xB1>(f1); f2 ^= dppu<0xB1>(f2); f3 ^= dppu<0xB1>(f3);
        f0 ^= dppu<0x4E>(f0); f1 ^= dppu<0x4E>(f1); f2 ^= dppu<0x4E>(f2); f3 ^= dppu<0x4E>(f3);
        f0 ^= dppu<0x141>(f0); f1 ^= dppu<0x141>(f1); f2 ^= dppu<0x141>(f2); f3 ^= dppu<0x141>(f3);
        if (allok((((f0 ^ xr[0]) | (f1 ^ xr[1])) | ((f2 ^ xr[2]) | (f3 ^ xr[3]))) == 0u) || ++spins > SPIN_CAP) break;
      }
      STAMPV(13, spins);
      STAMP(9);
      float c[8];
#pragma unroll
      for (int j = 0; j < 4; ++j) {
        c[2 * j] = fmaf(wr[3], blo(d3[j]), fmaf(wr[2], blo(d2[j]), fmaf(wr[1], blo(d1[j]), wr[0] * blo(d0[j]))));
        c[2 * j + 1] = fmaf(wr[3], bhi(d3[j]), fmaf(wr[2], bhi(d2[j]), fmaf(wr[1], bhi(d1[j]), wr[0] * bhi(d0[j]))));
      }
      const float b0 = swap32add(c[0], c[4]), b1 = swap32add(c[1], c[5]), b2 = swap32add(c[2], c[6]), b3 = swap32add(c[3], c[7]);
      const float e0 = swap16add(b0, b2), e1 = swap16add(b1, b3);
      const bool hi8 = (lane & 8) != 0;
      const float fin = (hi8 ? e1 : e0) + dppf<0x128>(hi8 ? e0 : e1);
      *(LAS u16*)(L + L_ACT + b * 400 + (64 * ch + 32 * (g8 & 1) + 16 * (g8 >> 2) + 8 * (((g8 >> 1) ^ h) & 1) + (lane & 7)) * 2) = (u16)pk2(fin, fin);
    }
    BAR();
    {
      v4u af[4];
#pragma unroll
      for (int s = 0; s < 4; ++s) af[s] = ldsr128(L, actr + s * 64);
      SB0();
      f32x4 acc = {0.f, 0.f, 0.f, 0.f};
#pragma unroll
      for (int s = 0; s < 4; ++s) acc = mfma16(af[s], uv[s], acc);
      const unsigned off = W_CTX + (h * 4 + qq) * 2048 + (16 * w + n16) * 16;
      const v4u cv = settag(acc, tg);
      if (lane < 16) GST4(off, cv, ws, 0, SC1);
    }
    STAMP(10);
    if (qq == 0) {
      const unsigned off = W_OUTP + ((h >> 3) * 32 + 4 * w + g) * 2048 + (16 * (h & 7) + n16) * 16;
      v4u o;
      unsigned spins = 0;
      for (;;) {
        GLD4(o, off, ws, 0, SC1);
        VMW1(0, o);
        if (allok(badtag(o, tg) == 0u) || ++spins > SPIN_CAP) break;
      }
      const f32x4 s = untag(o);
      *(LAS float*)(L + lane * 48 + w * 4) = tred4(s[0], s[1], s[2], s[3]);
      BAR();
    }
    if (qq == 0 && w == 0) {
      const f32x4 s0 = *(LAS f32x4*)(L + lane * 48), s1 = *(LAS f32x4*)(L + lane * 48 + 16);
      const float r = ((s0[0] + s0[1]) + (s0[2] + s0[3])) + ((s1[0] + s1[1]) + (s1[2] + s1[3]));
      const unsigned oo = (g * 512 + 128 * (h >> 3) + 16 * (h & 7) + n16) * 2;
      const unsigned rb = pk2(r, r);
      GSTH(oo, rb, a.out, 0, NOF);
      if (h == 0) {
        const v2u kv = {k0, k1};
        const unsigned z = 0;
        if (lane == 0) GST2(z, kv, ws, W_E, SC1);
      }
      STAMP(11);
    }
  } else {
    const int b = xc >> 1, p = 32 * b + (xc & 1) * 16 + ii;
    const int ht = w & 1, kq = w >> 1;
    const u16* cb = a.ckv + (size_t)p * 65536;
    const unsigned cv = lane * 16 + (w & 3) * 32768, cm = (w & 3) * 8 * CROW;
#define CDMA(n) DMA16(cv, cb + ((n) >> 2) * 2048, cm + ((n) >> 3) * CTILE + ((n) & 7) * CROW - ((n) & 3) * 1024, ((n) & 3) * 1024)
    {
      const int q4 = n16 >> 2, pp = lane & 3;
      unsigned base = L_WQ + (8 * g + q4) * 256 + (((2 * w + (pp >> 1)) ^ (2 * (q4 | ((g & 1) << 2)))) * 16) + 8 * (pp & 1);
      unsigned qpo = W_QP + (h * 4 + qq) * 2048 + (16 * w + n16) * 16;
      PIN(xb); PIN(base); PIN(qpo);
      if (K_PRE && w >= 4) {
#pragma unroll
        for (int n = 0; n < K_PRE; ++n) { VMW(K_PWIN - 1); CDMA(n); }
        VMW(K_PRE);
      } else {
        VMW(0);
      }
      BAR();
      STAMP(1);
      v4u bf[4], xf[4];
#pragma unroll
      for (int s = 0; s < 4; ++s) { xf[s] = ldsr128(L, xb ^ (s * 64)); bf[s] = ldstr(L, base + s * 8192, base + s * 8192 + 1024); }
      KEY_GET();
      SB0();
      f32x4 acc = {0.f, 0.f, 0.f, 0.f};
#pragma unroll
      for (int s = 0; s < 4; ++s) acc = mfma16(xf[s], bf[s], acc);
      const v4u gv = settag(acc, tg);
      if (lane < 16) GST4(qpo, gv, ws, 0, SC1);
    }
    const unsigned h1o = W_H1 + lane * 16;
#define H1OK(v) allok((((v.x ^ k0) | (v.y ^ k1)) | ((v.z ^ k0) | (v.w ^ k1))) == 0u)
    if (w >= 4) {
      SLEEP(K_CSLEEP, 1);
#pragma unroll
      for (int n = K_PRE; n < 8; ++n) CDMA(n);
    }
    BAR();
    if (w >= 4) {
#pragma unroll
      for (int n = 8; n < 32; ++n) {
        CDMA(n);
        if (n == K_HPOS && w == 4) {
          const v4u dv = {k0, k0, k0, k0};
          const unsigned dn = W_NEAR + p * 128 + lane * 16;
          if (lane < 8) GST4(dn, dv, ws, 0, SC1);
        }
      }
    } else if (w == 0) {
      SLEEP(K_H1SLEEP, 2);
      v4u h1;
      GLD4(h1, h1o, ws, 0, SC1);
      VMW1(0, h1);
      if (H1OK(h1)) *(LAS unsigned*)(L + L_FL) = 1u;
    }
    STAMP(2);
    unsigned qk_a = (16 * kq + 4 * (n16 & 3) + (n16 >> 2)) * CROW + g * 256, qk_b = qk_a + 2 * CTILE;
    unsigned pv_a = (2 * (g >> 1) + 4 * (n16 >> 2)) * CROW + w * 128 + (g & 1) * 32 + (lane & 3) * 8, pv_b = pv_a + 2 * CTILE;
    unsigned pw_off = L_PB + (kq >> 1) * PTILE + (16 * ht + n16) * 80 + (kq & 1) * 32 + g * 8, pr_off = L_PB + (lane & 31) * 80 + (lane >> 5) * 16;
    unsigned mw_off = L_MX + (16 * ht + n16) * 16 + kq * 4, mr_off = L_MX + (16 * ht + n16) * 16;
    unsigned qao = W_QA + b * 32768 + w * 4096 + lane * 16, qiw = w * 4096 + lane * 16, qir = ht * 16384 + lane * 16;
    PIN(qk_a); PIN(qk_b); PIN(pv_a); PIN(pv_b); PIN(pw_off); PIN(pr_off); PIN(mw_off); PIN(mr_off); PIN(qao); PIN(qiw); PIN(qir);
#define RV(F, t, c) F[c] = ldstr(L, ((t) < 2 ? pv_a : pv_b) + ((t) & 1) * CTILE + ((c) >> 1) * 16 * CROW + ((c) & 1) * 64, \
                                   ((t) < 2 ? pv_a : pv_b) + ((t) & 1) * CTILE + ((c) >> 1) * 16 * CROW + ((c) & 1) * 64 + CROW)
    VMW(16);
    BAR();
    STAMP(4);
    if (__builtin_amdgcn_readfirstlane(*(LAS unsigned*)(L + L_FL)) == 0u) {
      v4u h1;
      unsigned spins = 0;
      do { GLD4(h1, h1o, ws, 0, SC1); VMW1(0, h1); } while (!H1OK(h1) && ++spins <= SPIN_CAP);
    }
    v4u qa[16], fk[16], f0[4], f1[4], f2[4], f3[4];
    {
      v4u t0, t1, t2, t3;
#define QAG() do { GLD4(t0, qao, ws, 0, SC1); GLD4(t1, qao, ws, 1024, SC1); GLD4(t2, qao, ws, 2048, SC1); GLD4(t3, qao, ws, 3072, SC1); } while (0)
#define QAOK() allok(max(max(umax4(t0), umax4(t1)), max(umax4(t2), umax4(t3))) != ~0u)
      unsigned tries = 1;
      QAG();
#pragma unroll
      for (int s = 0; s < 16; ++s) fk[s] = ldsr128(L, qk_a + s * 16);
#pragma unroll
      for (int c = 0; c < 4; ++c) { RV(f0, 0, c); RV(f1, 1, c); }
      SB0();
      BAR();
      VMW4(0, t0, t1, t2, t3);
      STAMP(3);
      while (!QAOK() && ++tries <= SPIN_CAP) { SLEEP(K_BACKOFF, 7); QAG(); VMW4(0, t0, t1, t2, t3); }
      *(LAS v4u*)(L + qiw) = t0; *(LAS v4u*)(L + qiw + 1024) = t1; *(LAS v4u*)(L + qiw + 2048) = t2; *(LAS v4u*)(L + qiw + 3072) = t3;
      STAMPV(12, tries);
    }
    CST(0);
    BAR(); CST(1);
    STAMP(11);
    f32x4 SA, SB;
    f32x16 O0, O1;
    float la, lb, amax;
#define PWR(S, R, m, sum) do { const float p0_ = __builtin_amdgcn_exp2f(S[0] - (m)), p1_ = __builtin_amdgcn_exp2f(S[1] - (m)), p2_ = __builtin_amdgcn_exp2f(S[2] - (m)), \
                                           p3_ = __builtin_amdgcn_exp2f(S[3] - (m)); \
      *(LAS v2u*)(L + pw_off + (R) * 2 * PTILE) = v2u{pk2(p0_, p1_), pk2(p2_, p3_)}; sum = (p0_ + p1_) + (p2_ + p3_); } while (0)
#define SUMS() do { float l_ = la + lb; l_ = swap16add(l_, l_); l_ = swap32add(l_, l_); *(LAS float*)(L + mw_off + (L_LS - L_MX)) = l_; } while (0)
#define PRD(P, t) do { P[0] = ldsr128(L, pr_off + (t) * PTILE); P[1] = ldsr128(L, pr_off + (t) * PTILE + 32); } while (0)
#define NOP_(F, t, i) do {} while (0)
#define PV(P, F, t, RD, G, u) do { \
      RD(G, u, 0); if (t) MF32(O0, P[0], F[0]); else MF32Z(O0, P[0], F[0]); SB0(); \
      RD(G, u, 1); if (t) MF32(O1, P[0], F[1]); else MF32Z(O1, P[0], F[1]); SB0(); \
      RD(G, u, 2); MF32(O0, P[1], F[2]); SB0(); RD(G, u, 3); MF32(O1, P[1], F[3]); SB0(); } while (0)
#pragma unroll
    for (int s = 0; s < 6; ++s) qa[s] = ldsr128(L, qir + s * 1024);
    SB0();
#pragma unroll
    for (int s = 0; s < 16; ++s) {
      if (s + 6 < 16) qa[s + 6] = ldsr128(L, qir + (s + 6) * 1024);
      if (s) MF16(SA, fk[s], qa[s]); else MF16Z(SA, fk[s], qa[s]);
      if (s >= 10) fk[s - 10] = ldsr128(L, qk_b + (s - 10) * 16);
      SB0();
    }
    CST(2);
#pragma unroll
    for (int s = 0; s < 16; ++s) {
      if (s + 6 < 16) fk[s + 6] = ldsr128(L, qk_b + (s + 6) * 16);
      if (s) MF16(SB, fk[s], qa[s]); else MF16Z(SB, fk[s], qa[s]);
      if (s == 4) { PIN(SA); PWR(SA, 0, 0.f, la); }
      if (s == 8) { amax = absmax3(SA[0], SA[1], SA[2]); amax = absmax3(amax, SA[3], SA[3]); }
      SB0();
    }
    asm volatile("s_nop 7" : "+v"(SB));
    PWR(SB, 1, 0.f, lb);
    SB0();
    {
      amax = absmax3(amax, SB[0], SB[1]); amax = absmax3(amax, SB[2], SB[3]);
      if (__builtin_amdgcn_ballot_w64(!(amax <= 64.f)) != 0ull) *(LAS unsigned*)(L + L_FL + 4) = 1u;
      BAR(); CST(3);
      STAMP(5);
      v4u p0[2], p1[2], p2[2], p3[2];
      const unsigned fl = *(LAS unsigned*)(L + L_FL + 4);
      PRD(p0, 0); PRD(p1, 1); PRD(p2, 2); PRD(p3, 3);
      SB0();
      if (__builtin_amdgcn_readfirstlane(fl) != 0u) {
        const float hi = fmaxf(fmaxf(fmaxf(SA[0], SA[1]), fmaxf(SA[2], SA[3])), fmaxf(fmaxf(SB[0], SB[1]), fmaxf(SB[2], SB[3])));
        *(LAS float*)(L + mw_off) = swap32max(swap16max(hi));
        BAR();
        const f32x4 mv = *(LAS f32x4*)(L + mr_off);
        const float mx = fmaxf(fmaxf(mv[0], mv[1]), fmaxf(mv[2], mv[3]));
        *(LAS float*)(L + L_M + (16 * ht + n16) * 4) = mx;
        PWR(SA, 0, mx, la); PWR(SB, 1, mx, lb);
        BAR();
        PRD(p0, 0); PRD(p1, 1); PRD(p2, 2); PRD(p3, 3);
      }
      SB0();
      PV(p0, f0, 0, RV, f2, 2);
      PV(p1, f1, 1, RV, f3, 3);
      PV(p2, f2, 2, NOP_, f2, 2);
      SUMS(); SB0();
      PV(p3, f3, 3, NOP_, f2, 2);
    }
    unsigned ino = W_INBOX + (((lane >> 3) * 4 + kq) * 128 + p) * 256 + ht * 128 + (lane & 7) * 16;
    asm volatile("s_nop 7\n\ts_nop 3" : "+v"(O0), "+v"(O1), "+v"(ino));
    CST(4);
    STAMP(6);
    {
      const bool b3 = lane & 8;
      unsigned y[4];
#pragma unroll
      for (int r = 0; r < 4; ++r) {
        const auto s0 = __builtin_amdgcn_permlane16_swap(pk2(O0[4 * r], O1[4 * r]), pk2(O0[4 * r + 2], O1[4 * r + 2]), false, false);
        const auto s1 = __builtin_amdgcn_permlane16_swap(pk2(O0[4 * r + 1], O1[4 * r + 1]), pk2(O0[4 * r + 3], O1[4 * r + 3]), false, false);
        const v4u o = {b3 ? s1[0] : s0[0], dppu<0x128>(b3 ? s0[0] : s1[0]), b3 ? s1[1] : s0[1], dppu<0x128>(b3 ? s0[1] : s1[1])};
        const unsigned of = ino + r * 1048576;
        GST4(of, o, ws, 0, SC1);
        y[r] = fold4(o);
      }
      const bool b0 = lane & 1, b1 = lane & 2;
      const unsigned t01 = (b0 ? y[1] : y[0]) ^ dppu<0xB1>(b0 ? y[0] : y[1]), t23 = (b0 ? y[3] : y[2]) ^ dppu<0xB1>(b0 ? y[2] : y[3]);
      unsigned u = (b1 ? t23 : t01) ^ dppu<0x4E>(b1 ? t01 : t23);
      u ^= dppu<0x124>(u);
      if (lane & 4) *(LAS unsigned*)(L + L_RC + (8 * (lane & 3) + (lane >> 3)) * 32 + w * 4) = u;
    }
    CST(5);
    BAR();
    if (w < 2) {
      const int hh = 16 * w + (lane >> 2);
      const v2u ck = *(LAS v2u*)(L + L_RC + hh * 32 + (lane & 3) * 8);
      const f32x4 l4 = *(LAS f32x4*)(L + L_LS + hh * 16);
      const unsigned mref = *(LAS unsigned*)(L + L_M + hh * 4), lsum = __float_as_uint((l4[0] + l4[1]) + (l4[2] + l4[3]));
      const unsigned mix = mref ^ rot16(lsum);
      const v4u rec = {mref, lsum, ck.x ^ mix ^ k0, ck.y ^ mix ^ k1};
      const unsigned ro = W_REC + (hh * 128 + p) * 64 + (lane & 3) * 16;
      GST4(ro, rec, ws, 0, SC1);
    } else if (w == 2) {
      const v4u dv = {k0, k0, k0, k0};
      const unsigned dn = W_DONE + p * 128 + lane * 16;
      if (lane < 8) GST4(dn, dv, ws, 0, SC1);
    }
    CST(6); CST(7); CST(8); CST(9); CST(10); CST(11);
    STAMP(7);
    SLEEP(K_ASLEEP, 5);
    WODMA();
    if (w < 4) {
      v4u xv, gv;
      const unsigned xo = w * 1024 + lane * 16, go = lane * 16;
      GLD4(xv, xo, a.x, 0, NOF); GLD4(gv, go, a.gw, 0, NOF);
      const unsigned fl = 16 * w + n16, f = 56 * h + min(fl, 55u);
      const unsigned ho = W_HP + ((f >> 6) * 4 + g) * 2048 + (f & 63) * 16;
      v4u g0, u0;
      GLD4(g0, ho, ws, 0, SC1); GLD4(u0, ho, ws, 1024, SC1);
      VMW4(0, xv, gv, g0, u0);
      float s = 0.f;
#pragma unroll
      for (int j = 0; j < 4; ++j) s += rbf(blo(xv[j]) * blo(gv[j])) + rbf(bhi(xv[j]) * bhi(gv[j]));
      s = rowsum16(s); s = swap16add(s, s); s = swap32add(s, s);
      const float gt = rbf(__builtin_amdgcn_rcpf(1.f + __expf(-rbf(s))));
      if (lane == 0) { *(LAS float*)(L + L_GATE + w * 4) = gt; *(LAS float*)(L + L_GATE + 16 + w * 4) = rbf(1.f - gt); }
      unsigned spins = 0;
      while (!allok((badtag(g0, tg) | badtag(u0, tg)) == 0u) && ++spins <= SPIN_CAP) { GLD4(g0, ho, ws, 0, SC1); GLD4(u0, ho, ws, 1024, SC1); VMW2(0, g0, u0); }
      const f32x4 gf = untag(g0), uf = untag(u0);
      const float gg = rbf(tred4(gf[0], gf[1], gf[2], gf[3]));
      const float uu = rbf(tred4(uf[0], uf[1], uf[2], uf[3]));
      const float hv = rbf(gg * __builtin_amdgcn_rcpf(1.f + __expf(-gg))) * uu;
      *(LAS u16*)(L + L_ACT + g * 400 + 256 + fl * 2) = fl < 56 ? (u16)pk2(hv, hv) : (u16)0;
    }
    VMW(0);
    BAR();
    STAMP(8);
    v4u wo[4];
    f32x4 accd = {0.f, 0.f, 0.f, 0.f};
    {
      const int q = n16 >> 2, pp = lane & 3;
      const unsigned base = (8 * g + q) * 256 + (((2 * w + (pp >> 1)) ^ (2 * (q | ((g & 1) << 2)))) * 16) + 8 * (pp & 1);
#pragma unroll
      for (int s = 0; s < 4; ++s) wo[s] = ldstr(L, L_WO + base + s * 8192, L_WO + base + s * 8192 + 1024);
      const unsigned b1 = L_WD + base + (g == 3 ? 4096 : 8192);
      accd = mfma16(ldsr128(L, L_ACT + (lane & 3) * 400 + 256 + g * 16), ldstr(L, L_WD + base, L_WD + base + 1024), accd);
      accd = mfma16(ldsr128(L, L_ACT + (lane & 3) * 400 + 256 + g * 16 + 64), ldstr(L, b1, b1 + 1024), accd);
      accd *= *(LAS f32x4*)(L + L_GATE + 16);
    }
    const f32x4 gate = *(LAS f32x4*)(L + L_GATE);
    {
      const unsigned off = W_CTX + (h * 4 + g) * 2048 + (16 * w + n16) * 16;
      v4u c0;
      unsigned spins = 0;
      for (;;) {
        GLD4(c0, off, ws, 0, SC1);
        VMW1(0, c0);
        if (allok(badtag(c0, tg) == 0u) || ++spins > SPIN_CAP) break;
      }
      STAMP(9);
      const f32x4 cf = untag(c0);
      const float cvv = tred4(cf[0], cf[1], cf[2], cf[3]);
      *(LAS u16*)(L + L_ACT + g * 400 + (16 * w + n16) * 2) = (u16)pk2(cvv, cvv);
    }
    BAR();
    {
      v4u af[4];
#pragma unroll
      for (int s = 0; s < 4; ++s) af[s] = ldsr128(L, L_ACT + (lane & 3) * 400 + g * 16 + s * 64);
      SB0();
      f32x4 acc = {0.f, 0.f, 0.f, 0.f};
#pragma unroll
      for (int s = 0; s < 4; ++s) acc = mfma16(af[s], wo[s], acc);
      const v4u ov = settag(acc * gate + accd, tg);
      const unsigned off = W_OUTP + (qq * 32 + h) * 2048 + (16 * w + n16) * 16;
      if (lane < 16) GST4(off, ov, ws, 0, SC1);
    }
    STAMP(10);
  }
  asm volatile(".Lpf_end:");
}

extern "C" void kernel_launch(void* const* d_in, const int* in_sizes, int n_in, void* d_out, int out_size, void* d_ws, size_t ws_size,
                              hipStream_t stream) {
  Args a;
  a.x = (const u16*)d_in[0]; a.ckv = (const u16*)d_in[1]; a.wq = (const u16*)d_in[2]; a.wuk = (const u16*)d_in[3];
  a.wuv = (const u16*)d_in[4]; a.wo = (const u16*)d_in[5]; a.wg = (const u16*)d_in[7]; a.wu = (const u16*)d_in[8];
  a.wd = (const u16*)d_in[9]; a.gw = (const u16*)d_in[10]; a.out = (u16*)d_out; a.ws = (unsigned char*)d_ws;
  hipLaunchKernelGGL(fused_decode, dim3(256), dim3(512), 0, stream, a);
}
```

```cpp
#include <hip/hip_runtime.h>
#include <stdint.h>

typedef unsigned short u16;
typedef unsigned long long u64;
typedef unsigned v4u __attribute__((ext_vector_type(4)));
typedef unsigned v2u __attribute__((ext_vector_type(2)));
typedef float f32x4 __attribute__((ext_vector_type(4)));
typedef float f32x2 __attribute__((ext_vector_type(2)));
typedef float f32x16 __attribute__((ext_vector_type(16)));
typedef short s16x4 __attribute__((ext_vector_type(4)));
typedef short s16x8 __attribute__((ext_vector_type(8)));
typedef __bf16 bf16x8 __attribute__((ext_vector_type(8)));
typedef __bf16 bf16x2 __attribute__((ext_vector_type(2)));
#define LAS __attribute__((address_space(3)))
#define DI __device__ __forceinline__

constexpr int K_QGSLEEP = 27;
constexpr int K_STSLEEP = 20;
constexpr int K_GUARD = 228;
constexpr int K_QWAIT = 275;
constexpr int K_DGATHER = 228;
constexpr int K_HOLD = 480;
constexpr int K_DGS = 342;
constexpr int K_DOUT = 231;
constexpr float K_WINDOW = 32.f;
constexpr unsigned SPIN_CAP = 1u << 17;

constexpr unsigned W_E = 0;
constexpr unsigned W_START = 1024;
constexpr unsigned W_DONE = 16384;
constexpr unsigned W_GS = 32768;
constexpr unsigned W_QA = 65536;
constexpr unsigned W_HP = W_QA + 163840;
constexpr unsigned W_QP = W_HP + 262144;
constexpr unsigned W_REC = W_QP + 131072;
constexpr unsigned W_INBOX = 1048576, INS = 32768 + 640;
constexpr unsigned W_OUT2 = W_INBOX + 128 * INS;
constexpr unsigned W_END = W_OUT2 + 128 * 10240;
static_assert(W_REC + 262144 <= W_INBOX && W_END <= (8u << 20), "ws");

constexpr unsigned CROW = 1040, CTILE = 32 * CROW;
constexpr unsigned L_WQ = 3 * CTILE;
constexpr unsigned L_PB = 128 * CROW, PTILE = 32 * 80;
constexpr unsigned L_MX = L_PB + 4 * PTILE;
constexpr unsigned L_LS = L_MX + 512;
constexpr unsigned L_RC = L_LS + 512;
constexpr unsigned L_SINK = L_RC + 1024;
constexpr unsigned L_M = L_SINK + 128;
constexpr unsigned L_FL = L_M + 128;
constexpr unsigned L_ACT = L_FL + 16;
constexpr unsigned L_GATE = L_ACT + 1600;
constexpr unsigned L_KEY = L_GATE + 32;
constexpr unsigned L_X = (L_KEY + 32 + 255) & ~255u;
constexpr unsigned L_WD = L_X + 1024;
constexpr unsigned L_ACT2 = L_PB;
constexpr unsigned L_END = L_WD + 56 * 256;
static_assert(L_END <= 163840 && (L_ACT % 16) == 0 && (L_WQ % 256) == 0, "LDS");

struct Args {
  const u16 *wq, *x; unsigned char* ws; const u16 *ckv, *wuk, *wuv, *wo, *wg, *wu, *wd, *gw; u16* out;
};

#define DMA16(voff, sbase, m0v, IMM) \
  asm volatile("s_mov_b32 m0, %2\n\ts_nop 0\n\tglobal_load_lds_dwordx4 %0, %1 offset:%3" ::"v"(voff), "s"(sbase), "s"(m0v), "i"(IMM) : "memory")
#define GLD4(dst, voff, sbase, IMM, FL) \
  asm volatile("global_load_dwordx4 %0, %1, %2 offset:%3" FL : "=v"(dst) : "v"(voff), "s"(sbase), "i"(IMM) : "memory")
#define GLD2(dst, voff, sbase, IMM, FL) \
  asm volatile("global_load_dwordx2 %0, %1, %2 offset:%3" FL : "=v"(dst) : "v"(voff), "s"(sbase), "i"(IMM) : "memory")
#define GST4(voff, src, sbase, IMM, FL) \
  asm volatile("global_store_dwordx4 %0, %1, %2 offset:%3" FL "\n\ts_nop 1" ::"v"(voff), "v"(src), "s"(sbase), "i"(IMM) : "memory")
#define GST2(voff, src, sbase, IMM, FL) \
  asm volatile("global_store_dwordx2 %0, %1, %2 offset:%3" FL "\n\ts_nop 0" ::"v"(voff), "v"(src), "s"(sbase), "i"(IMM) : "memory")
#define GSTH(voff, src, sbase, IMM, FL) \
  asm volatile("global_store_short %0, %1, %2 offset:%3" FL ::"v"(voff), "v"(src), "s"(sbase), "i"(IMM) : "memory")
#define SC1 " sc1"
#define NOF ""
#define VMW(N) asm volatile("s_waitcnt vmcnt(%0)" ::"i"(N) : "memory")
#define VMW1(N, a) asm volatile("s_waitcnt vmcnt(%1)" : "+v"(a) : "i"(N) : "memory")
#define VMW2(N, a, b) asm volatile("s_waitcnt vmcnt(%2)" : "+v"(a), "+v"(b) : "i"(N) : "memory")
#define VMW4(N, a, b, c, d) asm volatile("s_waitcnt vmcnt(%4)" : "+v"(a), "+v"(b), "+v"(c), "+v"(d) : "i"(N) : "memory")
#define BAR() asm volatile("s_waitcnt lgkmcnt(0)\n\ts_barrier" ::: "memory")

#define WAITRT(t0, K) do { for (int z_ = 0; z_ < 400 && (int)(rt32() - (t0) - (unsigned)(K)) < 0; ++z_) {} } while (0)

DI f32x4 mfma16(v4u a, v4u b, f32x4 c) {
  return __builtin_amdgcn_mfma_f32_16x16x32_bf16(__builtin_bit_cast(bf16x8, a), __builtin_bit_cast(bf16x8, b), c, 0, 0, 0);
}
DI unsigned pk2(float lo, float hi) { f32x2 t = {lo, hi}; return __builtin_bit_cast(unsigned, __builtin_convertvector(t, bf16x2)); }
DI float rbf(float v) { return __uint_as_float(pk2(v, v) << 16); }
DI float blo(unsigned u) { return __uint_as_float(u << 16); }
DI float bhi(unsigned u) { return __uint_as_float(u & 0xffff0000u); }
template <int CTRL> DI float dppf(float x) { return __builtin_bit_cast(float, __builtin_amdgcn_mov_dpp(__builtin_bit_cast(int, x), CTRL, 0xf, 0xf, true)); }
DI float rowmax16(float x) {
  float r;
  asm("s_nop 1\n\tv_max_f32_dpp %0, %1, %1 row_ror:8 row_mask:0xf bank_mask:0xf\n\ts_nop 1\n\t"
      "v_max_f32_dpp %0, %0, %0 row_ror:4 row_mask:0xf bank_mask:0xf\n\ts_nop 1\n\t"
      "v_max_f32_dpp %0, %0, %0 quad_perm:[2,3,0,1] row_mask:0xf bank_mask:0xf\n\ts_nop 1\n\t"
      "v_max_f32_dpp %0, %0, %0 quad_perm:[1,0,3,2] row_mask:0xf bank_mask:0xf\n\ts_nop 1" : "=&v"(r) : "v"(x));
  return r;
}
DI float absmax3(float a, float b, float c) { float r; asm("v_max3_f32 %0, |%1|, |%2|, |%3|" : "=v"(r) : "v"(a), "v"(b), "v"(c)); return r; }
#define MF16Z(acc, a, b) asm volatile("v_mfma_f32_16x16x32_bf16 %0, %1, %2, 0" : "=&v"(acc) : "v"(a), "v"(b))
#define MF16(acc, a, b) asm volatile("v_mfma_f32_16x16x32_bf16 %0, %1, %2, %0" : "+v"(acc) : "v"(a), "v"(b))
#define MF32Z(acc, a, b) asm volatile("v_mfma_f32_32x32x16_bf16 %0, %1, %2, 0" : "=&v"(acc) : "v"(a), "v"(b))
#define MF32(acc, a, b) asm volatile("v_mfma_f32_32x32x16_bf16 %0, %1, %2, %0" : "+v"(acc) : "v"(a), "v"(b))
#define PIN(x) asm volatile("" : "+v"(x))
#define SB0() __builtin_amdgcn_sched_barrier(0)
DI float rowsum16(float x) { x += dppf<0x128>(x); x += dppf<0x124>(x); x += dppf<0x4E>(x); return x + dppf<0xB1>(x); }
DI float swap32add(float a, float b) { auto s = __builtin_amdgcn_permlane32_swap(__float_as_uint(a), __float_as_uint(b), false, false); return __uint_as_float(s[0]) + __uint_as_float(s[1]); }
DI float swap16add(float a, float b) { auto s = __builtin_amdgcn_permlane16_swap(__float_as_uint(a), __float_as_uint(b), false, false); return __uint_as_float(s[0]) + __uint_as_float(s[1]); }
DI float swap32max(float a) { auto s = __builtin_amdgcn_permlane32_swap(__float_as_uint(a), __float_as_uint(a), false, false); return fmaxf(__uint_as_float(s[0]), __uint_as_float(s[1])); }
DI float swap16max(float a) { auto s = __builtin_amdgcn_permlane16_swap(__float_as_uint(a), __float_as_uint(a), false, false); return fmaxf(__uint_as_float(s[0]), __uint_as_float(s[1])); }
DI float tred4(float a0, float a1, float a2, float a3) { return swap16add(swap32add(a0, a2), swap32add(a1, a3)); }
struct Tag { unsigned t0, t1, t2, t3; };
DI Tag mktag(unsigned k0, unsigned k1) { return Tag{k0 & 0xfffu, (k0 >> 12) & 0xfffu, ((k0 >> 24) | (k1 << 8)) & 0xfffu, (k1 >> 4) & 0xfffu}; }
DI v4u settag(f32x4 v, Tag t) {
  return v4u{(__float_as_uint(v[0]) & ~0xfffu) | t.t0, (__float_as_uint(v[1]) & ~0xfffu) | t.t1, (__float_as_uint(v[2]) & ~0xfffu) | t.t2, (__float_as_uint(v[3]) & ~0xfffu) | t.t3};
}
DI unsigned badtag(v4u v, Tag t) { return (((v.x ^ t.t0) | (v.y ^ t.t1)) | ((v.z ^ t.t2) | (v.w ^ t.t3))) & 0xfffu; }
DI f32x4 untag(v4u v) { return f32x4{__uint_as_float(v.x & ~0xfffu), __uint_as_float(v.y & ~0xfffu), __uint_as_float(v.z & ~0xfffu), __uint_as_float(v.w & ~0xfffu)}; }
template <int CTRL> DI unsigned dppu(unsigned x) { return (unsigned)__builtin_amdgcn_mov_dpp((int)x, CTRL, 0xf, 0xf, true); }
DI unsigned rot16(unsigned x) { return (x << 16) | (x >> 16); }
DI unsigned sum4(v4u v) { return (v.x + v.y) + (v.z + v.w); }
DI unsigned wavesum(unsigned x) {
  x += dppu<0x128>(x); x += dppu<0x124>(x); x += dppu<0x4E>(x); x += dppu<0xB1>(x);
  return (__builtin_amdgcn_readlane(x, 0) + __builtin_amdgcn_readlane(x, 16)) + (__builtin_amdgcn_readlane(x, 32) + __builtin_amdgcn_readlane(x, 48));
}
DI unsigned rt32() { u64 t; asm volatile("s_memrealtime %0\n\ts_waitcnt lgkmcnt(0)" : "=s"(t)::"memory"); return (unsigned)t; }
DI bool allok(bool c) { return __builtin_amdgcn_ballot_w64(!c) == 0ull; }

DI v4u ldsr128(LAS unsigned char* L, unsigned off) { return *(LAS v4u*)(L + off); }
DI v4u ldstr(LAS unsigned char* L, unsigned off0, unsigned off1) {
  s16x4 a = __builtin_amdgcn_ds_read_tr16_b64_v4i16((LAS s16x4*)(L + off0));
  s16x4 b = __builtin_amdgcn_ds_read_tr16_b64_v4i16((LAS s16x4*)(L + off1));
  return __builtin_bit_cast(v4u, __builtin_shufflevector(a, b, 0, 1, 2, 3, 4, 5, 6, 7));
}

DI unsigned xw_base(unsigned img, int cw, int lane) {
  const int g = lane >> 4, q = (lane & 15) >> 2, p = lane & 3;
  return img + (8 * g + q) * 128 + (((2 * cw + (p >> 1)) ^ (2 * ((q >> 1) | ((g & 1) << 1)))) * 16) + 8 * (p & 1);
}
DI f32x4 xw128(LAS unsigned char* L, unsigned base, unsigned xb) {
  v4u bf[4], xf[4];
#pragma unroll
  for (int s = 0; s < 4; ++s) { xf[s] = ldsr128(L, xb ^ (s * 64)); bf[s] = ldstr(L, base + s * 4096, base + s * 4096 + 512); }
  __builtin_amdgcn_sched_barrier(0);
  f32x4 acc = {0.f, 0.f, 0.f, 0.f};
#pragma unroll
  for (int s = 0; s < 4; ++s) acc = mfma16(xf[s], bf[s], acc);
  return acc;
}

__global__ void __launch_bounds__(512, 2) fused_decode(Args a) {
  __shared__ __attribute__((aligned(16))) unsigned char smem[163840];
  LAS unsigned char* L = (LAS unsigned char*)smem;
  const int tid = threadIdx.x, lane = tid & 63, w = __builtin_amdgcn_readfirstlane(tid >> 6);
  const int bid = blockIdx.x, xc = bid & 7, ii = (bid >> 3) & 15, isA = bid >> 7;
  const int h = 4 * xc + (ii >> 2), qq = ii & 3;
  const int g = lane >> 4, n16 = lane & 15;
  unsigned char* const ws = a.ws;

  v2u ev;
  if (w == 7) { const unsigned z = 0; GLD2(ev, z, ws, W_E, SC1); }
  if (w == 3) {
    const unsigned xo = g * 1024 + (isA ? (qq >> 1) * 512 : qq * 256) + ((n16 ^ (4 * g)) * 16);
    DMA16(xo, a.x, L_X, 0);
    if (isA) DMA16(xo + 256, a.x, L_X + 1024, 0);
  }
  if (isA) {
    const unsigned o0 = (256 * (qq >> 1) + 32 * w + (lane >> 3)) * 8192 + h * 256 + (qq & 1) * 128, m = L_WQ + w * 4096;
    const unsigned c0 = ((lane & 7) ^ ((lane >> 3) & 2)) * 16, c1 = c0 ^ 64;
    DMA16(o0 + c0, a.wq, m, 0); DMA16(o0 + 65536 + c1, a.wq, m + 1024, 0); DMA16(o0 + 131072 + c0, a.wq, m + 2048, 0); DMA16(o0 + 196608 + c1, a.wq, m + 3072, 0);
  } else {
    const unsigned o0 = (256 * (w & 1) + 8 * (4 * qq + (w >> 1)) + g) * 8192 + h * 256, m = w * 4096;
    DMA16(o0 + ((n16 ^ g) * 16), a.wuk, m, 0); DMA16(o0 + 4 * 8192 + ((n16 ^ (4 + g)) * 16), a.wuk, m + 1024, 0);
    DMA16(o0 + 128 * 8192 + ((n16 ^ (8 + g)) * 16), a.wuk, m + 2048, 0); DMA16(o0 + 132 * 8192 + ((n16 ^ (12 + g)) * 16), a.wuk, m + 3072, 0);
  }
  u64 pc; unsigned len;
  asm volatile("s_getpc_b64 %0\n.Lpf_here:\n\ts_mov_b32 %1, fused_decode_text_end-.Lpf_here" : "=s"(pc), "=s"(len));
#define CODEPF(chunk, sink) do { const unsigned off_ = min((unsigned)((chunk) * 1024 + lane * 16), len - 16u) & ~3u; DMA16(off_, pc, sink, 0); } while (0)
  if (w == 2) CODEPF(bid >> 3, L_PB);
  unsigned k0, k1;
  unsigned xb = L_X + (lane & 3) * 320 + g * 16;
  Tag tg;
  if (w == 7) {
    VMW1(4, ev);
    const u64 e = ((u64)(unsigned)__builtin_amdgcn_readfirstlane(ev.y) << 32) | (unsigned)__builtin_amdgcn_readfirstlane(ev.x);
    const u64 k = e * 0x9E3779B97F4A7C15ull + 0x632BE59BD9B4E019ull;
    const v2u kv = {(unsigned)k, (unsigned)(k >> 32)};
    *(LAS v2u*)(L + L_KEY) = kv;
    *(LAS unsigned*)(L + L_M + min(lane, 35) * 4) = 0u;
  }
#define START_GET() do { const unsigned so_ = W_START + (lane & 7) * 128; v2u sv_; unsigned sp_ = 0; \
      for (;;) { GLD2(sv_, so_, ws, 0, SC1); VMW1(0, sv_); if (allok(sv_.x == k0) || ++sp_ > SPIN_CAP) break; __builtin_amdgcn_s_sleep(8); } \
      const unsigned now_ = rt32(); unsigned age_ = now_ - sv_.y, sum_ = age_; \
      age_ = min(age_, dppu<0x124>(age_)); age_ = min(age_, dppu<0x4E>(age_)); age_ = min(age_, dppu<0xB1>(age_)); \
      sum_ += dppu<0x124>(sum_); sum_ += dppu<0x4E>(sum_); sum_ += dppu<0xB1>(sum_); \
      *(LAS unsigned*)(L + L_FL) = now_ - age_; *(LAS unsigned*)(L + L_FL + 12) = now_ - (sum_ >> 3); } while (0)
#define KEY_GET() do { const v2u kv_ = *(LAS v2u*)(L + L_KEY); k0 = __builtin_amdgcn_readfirstlane(kv_.x); k1 = __builtin_amdgcn_readfirstlane(kv_.y); tg = mktag(k0, k1); } while (0)

#define TOUCH() do { const unsigned char* tp = ws + W_INBOX + (min(lane, 2) * 47 + 1) * INS - 128; \
      if (lane == 3) tp = (const unsigned char*)a.out; \
      if (lane == 10) tp = ws + W_END; \
      if (lane == 4) tp = (const unsigned char*)a.wo + h * 131072; \
      if (lane == 5) tp = (const unsigned char*)a.wd + h * 57344; \
      if (lane >= 6 && lane < 10) tp = (const unsigned char*)a.wuv + (lane - 6) * 1048576 + h * 256; \
      if (lane < 11) { asm volatile("s_mov_b32 m0, %1\n\ts_nop 0\n\tglobal_load_lds_dword %0, off" :: "v"(tp), "s"(L_SINK) : "memory"); } } while (0)

  if (!isA) {
    if (h < 28) {
      VMW(3);
      const u16* src = (w >> 2) ? a.wu : a.wg;
      const unsigned o1 = (128 * qq + 32 * (w & 3) + (lane >> 3)) * 3584 + h * 128;
      const unsigned c0 = ((lane & 7) ^ ((lane >> 3) & 2)) * 16, c1 = c0 ^ 64, m1 = 32768 + w * 4096;
      DMA16(o1 + c0, src, m1, 0); DMA16(o1 + 8 * 3584 + c1, src, m1 + 1024, 0);
      DMA16(o1 + 16 * 3584 + c0, src, m1 + 2048, 0); DMA16(o1 + 24 * 3584 + c1, src, m1 + 3072, 0);
    }
    v4u uk[4];
    {
      unsigned ub = (16 * w + n16) * 256 + ((g ^ n16) * 16);
      PIN(ub);
      if (h < 28) VMW(4); else VMW(0);
#pragma unroll
      for (int s = 0; s < 4; ++s) uk[s] = ldsr128(L, ub ^ (s * 64));
      BAR();
      KEY_GET();
      if (ii == 4 && w == 7) {
        const unsigned t = rt32();
        const v4u dv = {k0, t, k0, t};
        const unsigned dn = W_START + xc * 128 + lane * 16;
        if (lane < 8) GST4(dn, dv, ws, 0, SC1);
      }
      __builtin_amdgcn_s_sleep(K_QGSLEEP);
    }
    unsigned actr = L_ACT + (lane & 3) * 400 + g * 16;
    unsigned qsw = 65536 + (lane & 3) * 272 + (w >> 1) * 64 + (w & 1) * 32 + g * 8;
    PIN(actr); PIN(qsw);
    if (w < 4) {
      const unsigned off = W_QP + (h * 2 + (lane >> 5)) * 2048 + (32 * w + (lane & 31)) * 16;
      unsigned actw = L_ACT + (lane >> 5) * 800 + (32 * w + (lane & 31)) * 2;
      PIN(actw);
      v4u g0;
      unsigned spins = 0;
      for (;;) {
        GLD4(g0, off, ws, 0, SC1);
        VMW1(0, g0);
        if (allok(badtag(g0, tg) == 0u) || ++spins > SPIN_CAP) break;
        __builtin_amdgcn_s_sleep(4);
      }
      const f32x4 qp = untag(g0);
      const unsigned qv = pk2(swap32add(qp[0], qp[2]), swap32add(qp[1], qp[3]));
      *(LAS u16*)(L + actw) = (u16)qv; *(LAS u16*)(L + actw + 400) = (u16)(qv >> 16);
    }
    BAR();
    {
      v4u qf[4];
#pragma unroll
      for (int s = 0; s < 4; ++s) qf[s] = ldsr128(L, actr + s * 64);
      SB0();
      f32x4 acc = {0.f, 0.f, 0.f, 0.f};
#pragma unroll
      for (int s = 0; s < 4; ++s) acc = mfma16(uk[s], qf[s], acc);
      constexpr float SC = 0.08838834764831845f * 1.4426950408889634f;
      if (n16 < 4) *(LAS v2u*)(L + qsw) = v2u{pk2(acc[0] * SC, acc[1] * SC), pk2(acc[2] * SC, acc[3] * SC)};
      BAR();
      if (w == 0) {
        const v4u o = ldsr128(L, 65536 + g * 272 + n16 * 16);
        const unsigned off = W_QA + (((g * 2 + (h >> 4)) * 4 + qq) * 16 + (h & 15)) * 320 + n16 * 16;
        GST4(off, o, ws, 0, SC1);
        unsigned sm = (o.x + o.y) + (o.z + o.w);
        sm += dppu<0x128>(sm); sm += dppu<0x124>(sm); sm += dppu<0x4E>(sm); sm += dppu<0xB1>(sm);
        const v4u rc = {n16 == 0 ? k0 * (2 * (h & 15) + 1) - sm : 0u, 0u, 0u, 0u};
        if (n16 < 4) GST4(off, rc, ws, 256, SC1);
      }
    }
    if (w >= 4) {
      v4u xv, gv;
      const unsigned xo = (w - 4) * 1024 + lane * 16, go = lane * 16;
      GLD4(xv, xo, a.x, 0, NOF); GLD4(gv, go, a.gw, 0, NOF);
      VMW2(0, xv, gv);
      float s = 0.f;
#pragma unroll
      for (int j = 0; j < 4; ++j) s += rbf(blo(xv[j]) * blo(gv[j])) + rbf(bhi(xv[j]) * bhi(gv[j]));
      s = rowsum16(s); s = swap16add(s, s); s = swap32add(s, s);
      const float gt = rbf(__builtin_amdgcn_rcpf(1.f + __expf(-rbf(s))));
      if (lane == 0) { *(LAS float*)(L + L_GATE + (w - 4) * 4) = gt; *(LAS float*)(L + L_GATE + 16 + (w - 4) * 4) = rbf(1.f - gt); }
    }
    if (w == 1) START_GET();
    if (w == 2) TOUCH();
    BAR();
    WAITRT(__builtin_amdgcn_readfirstlane(*(LAS unsigned*)(L + L_FL + 12)), K_QWAIT);
    const unsigned wov = (h * 128 + 4 * w) * 1024;
#define WOQ(j) do { const unsigned o_ = wov + (((qq + (j)) & 3) * 32768) + ((lane ^ ((w & 2) << 2)) * 16), m_ = (j) * 32768 + w * 4096; \
      DMA16(o_, a.wo, m_, 0); DMA16((o_ ^ 32) + 1024, a.wo, m_ + 1024, 0); DMA16((o_ ^ 64) + 2048, a.wo, m_ + 2048, 0); DMA16((o_ ^ 96) + 3072, a.wo, m_ + 3072, 0); } while (0)
    WOQ(0);
    {
      const unsigned o0 = (128 * qq + 16 * w + g) * 8192 + h * 256;
      const unsigned ca = (n16 ^ (2 * g)) * 16, cb = ca ^ 128;
      const unsigned m = 98304 + w * 4096;
      DMA16(o0 + ca, a.wuv, m, 0);
      DMA16(o0 + 32768 + ca, a.wuv, m + 1024, 0);
      DMA16(o0 + 65536 + cb, a.wuv, m + 2048, 0);
      DMA16(o0 + 98304 + cb, a.wuv, m + 3072, 0);
    }
    v4u gu;
    if (h < 28) {
      f32x4 gf = xw128(L, xw_base(32768 + (w >> 2) * 16384, w & 3, lane), xb);
      if (w >= 4) gf *= *(LAS f32x4*)(L + L_GATE + 16);
      gu = settag(gf, tg);
    }
    BAR();
    VMW(4);
    if (h < 28) {
      const unsigned off = W_HP + ((h * 4 + qq) * 2 + (w >> 2)) * 1024 + (16 * (w & 3) + n16) * 16;
      if (lane < 16) GST4(off, gu, ws, 0, SC1);
    }
    WOQ(1); WOQ(2);
    if (h < 28) VMW(9); else VMW(8);
    BAR();
    v4u uv[4];
    {
      const int q = n16 >> 2, p = lane & 3;
      const unsigned base = 98304 + (8 * g + q) * 256 + (((2 * w + (p >> 1)) ^ (2 * (q | ((g & 1) << 2)))) * 16) + 8 * (p & 1);
#pragma unroll
      for (int s = 0; s < 4; ++s) uv[s] = ldstr(L, base + s * 8192, base + s * 8192 + 1024);
    }
    BAR();
    const unsigned ho = W_DONE + (16 * w + n16) * 128;
    v2u hv;
    if (w == 3) { CODEPF(ii, L_PB + 2048); CODEPF(ii + 16, L_PB + 2048); }
    GLD2(hv, ho, ws, 0, SC1);
    WOQ(3);
    VMW1(4, hv);
    const float gate = *(LAS float*)(L + L_GATE + (w >> 1) * 4);
    unsigned tlast;
    v4u wo[4][4];
    unsigned wob = (8 * g + (n16 >> 2)) * 1024 + (((8 * w + ((lane & 3) >> 1)) ^ (2 * ((n16 >> 2) | ((g & 1) << 2)))) * 16) + 8 * (lane & 1);
    unsigned a2w = L_ACT2 + g * 272 + (16 * w + n16) * 2, a2r = L_ACT2 + (n16 >> 2) * 272 + g * 16, a1r = L_ACT + (n16 >> 2) * 400 + g * 16;
    unsigned o2o = W_OUT2 + (g * 32 + 4 * w + (n16 >> 2)) * 10240 + (h * 4 + qq) * 64 + (lane & 3) * 16;
    PIN(a2w); PIN(a2r); PIN(a1r); PIN(o2o);
    unsigned wb[4], wc[4];
#pragma unroll
    for (int t = 0; t < 4; ++t) { wb[t] = wob ^ (t * 32); wc[t] = wb[t] + 65536; PIN(wb[t]); PIN(wc[t]); }
    {
      const int b = w >> 1, ch = w & 1, g8 = lane >> 3;
      unsigned so = W_REC + ((h * 128 + 32 * b + (lane & 31)) * 4 + qq) * 16;
      unsigned io = W_INBOX + (h * 4 + qq) * INS + (32 * b + g8) * 256 + ch * 128 + (lane & 7) * 16 + 4096;
      unsigned a1w = L_ACT + b * 400 + (64 * ch + 32 * (g8 & 1) + 16 * (g8 >> 2) + 8 * (((g8 >> 1) ^ h) & 1) + (lane & 7)) * 2;
      unsigned bp = g8 * 4;
      PIN(so); PIN(io); PIN(a1w); PIN(bp);
      unsigned spins = 0;
      {
        while (!allok(hv.x == k0) && ++spins <= SPIN_CAP) { __builtin_amdgcn_s_sleep(4); GLD2(hv, ho, ws, 0, SC1); VMW1(0, hv); }
        const unsigned now = rt32();
        unsigned age = now + 2048u - hv.y;
        age = min(age, dppu<0x128>(age)); age = min(age, dppu<0x124>(age)); age = min(age, dppu<0x4E>(age)); age = min(age, dppu<0xB1>(age));
        *(LAS unsigned*)(L + L_MX + w * 4) = now + 2048u - age;
        BAR();
        const v4u ta = ldsr128(L, L_MX), tb = ldsr128(L, L_MX + 16);
        const unsigned youngest = min(min(min(now - ta.x + 4096u, now - ta.y + 4096u), min(now - ta.z + 4096u, now - ta.w + 4096u)),
                                      min(min(now - tb.x + 4096u, now - tb.y + 4096u), min(now - tb.z + 4096u, now - tb.w + 4096u)));
        tlast = now + 4096u - __builtin_amdgcn_readfirstlane(youngest);
#define WOFRAG(j) _Pragma("unroll") for (int t = 0; t < 4; ++t) wo[j][t] = ldstr(L, ((j) < 2 ? wb[t] : wc[t]) + ((j) & 1) * 32768, ((j) < 2 ? wb[t] : wc[t]) + ((j) & 1) * 32768 + 4096)
        if (w == 7) {
          const unsigned tn = rt32(), tw = tlast + (unsigned)K_DGATHER, t = (int)(tn - tw) > 0 ? tn : tw;
          const v4u dv = {k0, t, k0, t};
          const unsigned dn = W_GS + bid * 128 + lane * 16;
          if (lane < 8) GST4(dn, dv, ws, 0, SC1);
        }
        WAITRT(tlast, K_DGATHER);
      }
      float c[8], inv;
      spins = 0;
      for (;;) {
        v4u rc, d0, d1, d2, d3;
        GLD4(rc, so, ws, 0, SC1);
        GLD4(d0, io, ws, -4096, SC1); GLD4(d1, io, ws, -2048, SC1); GLD4(d2, io, ws, 0, SC1); GLD4(d3, io, ws, 2048, SC1);
        if (spins == 0) { WOFRAG(0); WOFRAG(1); WOFRAG(2); SB0(); }
        VMW1(4, rc);
        const float ms = __uint_as_float(rc.x), ls = __uint_as_float(rc.y);
        unsigned ck = lane < 32 ? (ch ? rc.w : rc.z) + rc.x + rot16(rc.y) : 0u;
        const float mx = swap16max(rowmax16(ms));
        const float e = __builtin_amdgcn_exp2f(ms - mx);
        float wr[4];
#pragma unroll
        for (int r = 0; r < 4; ++r) wr[r] = __builtin_bit_cast(float, __builtin_amdgcn_ds_bpermute(bp + 32 * r, __builtin_bit_cast(int, e)));
        const float rs = rowsum16(e * ls);
        inv = gate * __builtin_amdgcn_rcpf(swap16add(rs, rs));
        SB0();
#define ACCUM(d, r) do { VMW1(3 - (r), d); ck += sum4(d); \
          _Pragma("unroll") for (int j = 0; j < 4; ++j) { \
            c[2 * j] = (r) ? fmaf(wr[r], blo(d[j]), c[2 * j]) : wr[r] * blo(d[j]); \
            c[2 * j + 1] = (r) ? fmaf(wr[r], bhi(d[j]), c[2 * j + 1]) : wr[r] * bhi(d[j]); } SB0(); } while (0)
        ACCUM(d0, 0); ACCUM(d1, 1); ACCUM(d2, 2); ACCUM(d3, 3);
        if (wavesum(ck) == k0 * 1024u || ++spins > SPIN_CAP) break;
        __builtin_amdgcn_s_sleep(4);
      }
      const float b0 = swap32add(c[0], c[4]), b1 = swap32add(c[1], c[5]), b2 = swap32add(c[2], c[6]), b3 = swap32add(c[3], c[7]);
      const float e0 = swap16add(b0, b2), e1 = swap16add(b1, b3);
      const bool hi8 = (lane & 8) != 0;
      const float fin = ((hi8 ? e1 : e0) + dppf<0x128>(hi8 ? e0 : e1)) * inv;
      *(LAS u16*)(L + a1w) = (u16)pk2(fin, fin);
    }
    BAR();
    {
      v4u af[4];
#pragma unroll
      for (int s = 0; s < 4; ++s) af[s] = ldsr128(L, a1r + s * 64);
      WOFRAG(3);
      SB0();
      f32x4 acc = {0.f, 0.f, 0.f, 0.f};
#pragma unroll
      for (int s = 0; s < 4; ++s) acc = mfma16(af[s], uv[s], acc);
      *(LAS u16*)(L + a2w) = (u16)pk2(acc[0], acc[0]);
    }
    BAR();
    {
      v4u af[4];
#pragma unroll
      for (int j = 0; j < 4; ++j) af[j] = ldsr128(L, a2r + ((qq + j) & 3) * 64);
      SB0();
      f32x4 o[4];
#pragma unroll
      for (int t = 0; t < 4; ++t) o[t] = f32x4{0.f, 0.f, 0.f, 0.f};
#pragma unroll
      for (int j = 0; j < 4; ++j)
#pragma unroll
        for (int t = 0; t < 4; ++t) o[t] = mfma16(af[j], wo[j][t], o[t]);
      const v4u ov = settag(f32x4{o[0][0], o[1][0], o[2][0], o[3][0]}, tg);
      GST4(o2o, ov, ws, 0, SC1);
    }
  } else {
    const int b = xc >> 1, p = 32 * b + (xc & 1) * 16 + ii;
    const int ht = w & 1, kq = w >> 1;
    const u16* cb = a.ckv + (size_t)p * 65536;
    const unsigned cv = lane * 16 + (w & 3) * 32768, cm = (w & 3) * 8 * CROW;
#define CDMA(n) DMA16(cv, cb + ((n) >> 2) * 2048, cm + ((n) >> 3) * CTILE + ((n) & 7) * CROW - ((n) & 3) * 1024, ((n) & 3) * 1024)
    unsigned qk_a = (16 * kq + 4 * (n16 & 3) + (n16 >> 2)) * CROW + g * 256, qk_b = qk_a + 2 * CTILE;
    unsigned pv_a = (2 * (g >> 1) + 4 * (n16 >> 2)) * CROW + w * 128 + (g & 1) * 32 + (lane & 3) * 8, pv_b = pv_a + 2 * CTILE;
    unsigned pw_off = L_PB + (kq >> 1) * PTILE + (16 * ht + n16) * 80 + (kq & 1) * 32 + g * 8, pr_off = L_PB + (lane & 31) * 80 + (lane >> 5) * 16;
    unsigned mw_off = L_MX + (16 * ht + n16) * 16 + kq * 4, mr_off = L_MX + (16 * ht + n16) * 16;
    unsigned qao = W_QA + (b * 8 + w) * 5120 + lane * 16, qir = ht * 16384 + g * 256 + ((n16 ^ g) * 16), qir1 = (qir ^ 64) + 1024;
    unsigned qiw[5];
#pragma unroll
    for (int i = 0; i < 5; ++i) {
      const unsigned n = 64 * i + lane, hd = (n * 3277) >> 16, j = n - 20 * hd;
      qiw[i] = j < 16 ? w * 4096 + j * 256 + ((hd ^ (j & 7)) * 16) : L_MX + (lane & 31) * 16;
      PIN(qiw[i]);
    }
    PIN(qk_a); PIN(qk_b); PIN(pv_a); PIN(pv_b); PIN(pw_off); PIN(pr_off); PIN(mw_off); PIN(mr_off); PIN(qao); PIN(qir); PIN(qir1);
    unsigned ino = W_INBOX + ((lane >> 3) * 4 + kq) * INS + p * 256 + ht * 128 + (lane & 7) * 16, rcw = L_RC + (8 * (lane & 3) + (lane >> 3)) * 32 + w * 4;
    PIN(ino); PIN(rcw);
    if (w < 4) {
      unsigned base = xw_base(L_WQ, w, lane);
      unsigned qpo = W_QP + (h * 2 + (qq >> 1)) * 2048 + (64 * (qq & 1) + 16 * w + n16) * 16;
      PIN(xb); PIN(base); PIN(qpo);
      VMW(0);
      BAR();
      v4u bf[8], xf[8];
#pragma unroll
      for (int s = 0; s < 8; ++s) { xf[s] = ldsr128(L, (xb ^ ((s & 3) * 64)) + (s >> 2) * 1024); bf[s] = ldstr(L, base + s * 4096, base + s * 4096 + 512); }
      KEY_GET();
      SB0();
      f32x4 acc = {0.f, 0.f, 0.f, 0.f}, acc2 = acc;
#pragma unroll
      for (int s = 0; s < 4; ++s) { acc = mfma16(xf[s], bf[s], acc); acc2 = mfma16(xf[s + 4], bf[s + 4], acc2); }
      const v4u gv = settag(acc + acc2, tg);
      if (lane < 16) GST4(qpo, gv, ws, 0, SC1);
      BAR();
      if (w == 0) { __builtin_amdgcn_s_sleep(K_STSLEEP); START_GET(); }
    } else {
#pragma unroll
      for (int n = 0; n < 4; ++n) { if (n) VMW(4); CDMA(n); }
      VMW(4);
      BAR();
      KEY_GET();
      BAR();
#pragma unroll
      for (int n = 4; n < 32; ++n) CDMA(n);
    }
#define RV(F, t, c) F[c] = ldstr(L, ((t) < 2 ? pv_a : pv_b) + ((t) & 1) * CTILE + ((c) >> 1) * 16 * CROW + ((c) & 1) * 64, \
                                   ((t) < 2 ? pv_a : pv_b) + ((t) & 1) * CTILE + ((c) >> 1) * 16 * CROW + ((c) & 1) * 64 + CROW)
    VMW(16);
    BAR();
    v4u qa[16], fk[16], f0[4], f1[4], f2[4], f3[4];
    unsigned tref;
    {
      v4u t0, t1, t2, t3, t4;
      const unsigned qao4 = qao + 4096;
#define QAG() do { GLD4(t0, qao, ws, 0, SC1); GLD4(t1, qao, ws, 1024, SC1); GLD4(t2, qao, ws, 2048, SC1); GLD4(t3, qao, ws, 3072, SC1); GLD4(t4, qao4, ws, 0, SC1); } while (0)
      unsigned tries = 1;
#pragma unroll
      for (int s = 0; s < 16; ++s) fk[s] = ldsr128(L, qk_a + s * 16);
#pragma unroll
      for (int c = 0; c < 4; ++c) { RV(f0, 0, c); RV(f1, 1, c); }
      SB0();
      BAR();
      tref = __builtin_amdgcn_readfirstlane(*(LAS unsigned*)(L + L_FL + 12));
      WAITRT(__builtin_amdgcn_readfirstlane(*(LAS unsigned*)(L + L_FL)), K_GUARD);
      for (;;) {
        QAG();
        unsigned ck;
        VMW1(4, t0); *(LAS v4u*)(L + qiw[0]) = t0; ck = sum4(t0); SB0();
        VMW1(3, t1); *(LAS v4u*)(L + qiw[1]) = t1; ck += sum4(t1); SB0();
        VMW1(2, t2); *(LAS v4u*)(L + qiw[2]) = t2; ck += sum4(t2); SB0();
        VMW1(1, t3); *(LAS v4u*)(L + qiw[3]) = t3; ck += sum4(t3); SB0();
        VMW1(0, t4); *(LAS v4u*)(L + qiw[4]) = t4; ck += sum4(t4); SB0();
        if (wavesum(ck) == k0 * 256u || ++tries > SPIN_CAP) break;
        __builtin_amdgcn_s_sleep(10);
      }
    }

    if (w == 0) {
      const unsigned tn = rt32(), th = tref + (unsigned)K_HOLD - 110u, t = (int)(tn - th) > 0 ? tn : th;
      const v4u dv = {k0, t, k0, t};
      const unsigned dn = W_DONE + p * 128 + lane * 16;
      if (lane < 8) GST4(dn, dv, ws, 0, SC1);
      *(LAS unsigned*)(L + L_FL + 8) = t;
    }
    BAR();
    f32x4 SA, SB;
    f32x16 O0, O1;
    float la, lb, amax;
    u64 t5;
#define PWR(S, R, m, sum) do { const float p0_ = __builtin_amdgcn_exp2f(S[0] - (m)), p1_ = __builtin_amdgcn_exp2f(S[1] - (m)), p2_ = __builtin_amdgcn_exp2f(S[2] - (m)), \
                                           p3_ = __builtin_amdgcn_exp2f(S[3] - (m)); \
      *(LAS v2u*)(L + pw_off + (R) * 2 * PTILE) = v2u{pk2(p0_, p1_), pk2(p2_, p3_)}; sum = (p0_ + p1_) + (p2_ + p3_); } while (0)
#define SUMS() do { float l_ = la + lb; l_ = swap16add(l_, l_); l_ = swap32add(l_, l_); *(LAS float*)(L + mw_off + (L_LS - L_MX)) = l_; } while (0)
#define PRD(P, t) do { P[0] = ldsr128(L, pr_off + (t) * PTILE); P[1] = ldsr128(L, pr_off + (t) * PTILE + 32); } while (0)
#define NOP_(F, t, i) do {} while (0)
#define PV(P, F, t, RD, G, u) do { \
      RD(G, u, 0); if (t) MF32(O0, P[0], F[0]); else MF32Z(O0, P[0], F[0]); SB0(); \
      RD(G, u, 1); if (t) MF32(O1, P[0], F[1]); else MF32Z(O1, P[0], F[1]); SB0(); \
      RD(G, u, 2); MF32(O0, P[1], F[2]); SB0(); RD(G, u, 3); MF32(O1, P[1], F[3]); SB0(); } while (0)
#pragma unroll
    for (int s = 0; s < 6; ++s) qa[s] = ldsr128(L, ((s & 1) ? qir1 : qir) + (s >> 1) * 2048);
    SB0();
#pragma unroll
    for (int s = 0; s < 16; ++s) {
      if (s + 6 < 16) qa[s + 6] = ldsr128(L, ((s & 1) ? qir1 : qir) + ((s + 6) >> 1) * 2048);
      if (s) MF16(SA, fk[s], qa[s]); else MF16Z(SA, fk[s], qa[s]);
      if (s >= 10) fk[s - 10] = ldsr128(L, qk_b + (s - 10) * 16);
      SB0();
    }
#pragma unroll
    for (int s = 0; s < 16; ++s) {
      if (s + 6 < 16) fk[s + 6] = ldsr128(L, qk_b + (s + 6) * 16);
      if (s) MF16(SB, fk[s], qa[s]); else MF16Z(SB, fk[s], qa[s]);
      if (s == 4) { PIN(SA); PWR(SA, 0, 0.f, la); }
      if (s == 8) { amax = absmax3(SA[0], SA[1], SA[2]); amax = absmax3(amax, SA[3], SA[3]); }
      SB0();
    }

    asm volatile("s_nop 7" : "+v"(SB));
    PWR(SB, 1, 0.f, lb);
    SB0();
    {
      amax = absmax3(amax, SB[0], SB[1]); amax = absmax3(amax, SB[2], SB[3]);
      if (__builtin_amdgcn_ballot_w64(!(amax <= K_WINDOW)) != 0ull) *(LAS unsigned*)(L + L_FL + 4) = 1u;
      asm volatile("s_memrealtime %0\n\ts_waitcnt lgkmcnt(0)\n\ts_barrier" : "=s"(t5)::"memory");
      v4u p0[2], p1[2], p2[2], p3[2];
      const unsigned fl = *(LAS unsigned*)(L + L_FL + 4);
      PRD(p0, 0); PRD(p1, 1); PRD(p2, 2); PRD(p3, 3);
      SB0();
      if (__builtin_amdgcn_readfirstlane(fl) != 0u) {
        const float hi = fmaxf(fmaxf(fmaxf(SA[0], SA[1]), fmaxf(SA[2], SA[3])), fmaxf(fmaxf(SB[0], SB[1]), fmaxf(SB[2], SB[3])));
        *(LAS float*)(L + mw_off) = swap32max(swap16max(hi));
        BAR();
        const f32x4 mv = *(LAS f32x4*)(L + mr_off);
        const float mx = fmaxf(fmaxf(mv[0], mv[1]), fmaxf(mv[2], mv[3]));
        *(LAS float*)(L + L_M + (16 * ht + n16) * 4) = mx;
        PWR(SA, 0, mx, la); PWR(SB, 1, mx, lb);
        BAR();
        PRD(p0, 0); PRD(p1, 1); PRD(p2, 2); PRD(p3, 3);
      }
      SB0();
      PV(p0, f0, 0, RV, f2, 2);
      PV(p1, f1, 1, RV, f3, 3);
      PV(p2, f2, 2, NOP_, f2, 2);
      PV(p3, f3, 3, NOP_, f2, 2);
    }
    asm volatile("s_nop 11" : "+v"(O0), "+v"(O1));

    {
      const bool b3 = lane & 8, b0 = lane & 1, b1 = lane & 2;
      v4u o[4];
      unsigned y[4];
#define PACK(r) do { \
        const auto s0 = __builtin_amdgcn_permlane16_swap(pk2(O0[4 * r], O1[4 * r]), pk2(O0[4 * r + 2], O1[4 * r + 2]), false, false); \
        const auto s1 = __builtin_amdgcn_permlane16_swap(pk2(O0[4 * r + 1], O1[4 * r + 1]), pk2(O0[4 * r + 3], O1[4 * r + 3]), false, false); \
        o[r] = v4u{b3 ? s1[0] : s0[0], dppu<0x128>(b3 ? s0[0] : s1[0]), b3 ? s1[1] : s0[1], dppu<0x128>(b3 ? s0[1] : s1[1])}; } while (0)
#define PUT(r) do { const unsigned of = ino + (r) * 32 * INS; GST4(of, o[r], ws, 0, SC1); } while (0)
#define LINESUMS() do { _Pragma("unroll") for (int r = 0; r < 4; ++r) y[r] = sum4(o[r]); \
        const unsigned t01 = (b0 ? y[1] : y[0]) + dppu<0xB1>(b0 ? y[0] : y[1]), t23 = (b0 ? y[3] : y[2]) + dppu<0xB1>(b0 ? y[2] : y[3]); \
        unsigned u = (b1 ? t23 : t01) + dppu<0x4E>(b1 ? t01 : t23); \
        u += dppu<0x124>(u); \
        if (lane & 4) *(LAS unsigned*)(L + rcw) = u; } while (0)
      if ((int)(tref + (unsigned)K_HOLD - (unsigned)t5) > 35) WAITRT(tref, K_HOLD);
      if (w < 4) {
        PACK(0); PUT(0); PACK(1); PUT(1); PACK(2); PUT(2); PACK(3); PUT(3);
        SUMS(); LINESUMS();

        BAR();
        if (w < 2) {
          const int hh = 16 * w + (lane >> 2);
          const v2u ck = *(LAS v2u*)(L + L_RC + hh * 32 + (lane & 3) * 8);
          const f32x4 l4 = *(LAS f32x4*)(L + L_LS + hh * 16);
          const unsigned mref = *(LAS unsigned*)(L + L_M + hh * 4), lsum = __float_as_uint((l4[0] + l4[1]) + (l4[2] + l4[3]));
          const unsigned mix = mref + rot16(lsum), odd = 2 * (p & 31) + 1;
          const v4u rec = {mref, lsum, k0 * odd - mix - ck.x, k0 * odd - mix - ck.y};
          const unsigned ro = W_REC + (hh * 128 + p) * 64 + (lane & 3) * 16;
          GST4(ro, rec, ws, 0, SC1);
        }
      } else {
        PACK(0); PUT(0); PACK(1); PACK(2); PACK(3);
        SUMS(); LINESUMS();

        BAR();
        PUT(1); PUT(2); PUT(3);
      }
    }

    {
      if (w < 7) {
        const unsigned o1 = ((56 * h + 8 * w + g) * 1024 + qq * 256 + (n16 ^ (2 * g)) * 16) ^ ((w & 1) * 128);
        DMA16(o1, a.wd, L_WD + w * 2048, 0); DMA16(o1 + 4096, a.wd, L_WD + w * 2048 + 1024, 0);
      }
      if (w < 4) {
        const unsigned fl = 16 * w + n16, f = 56 * h + min(fl, 55u);
        const unsigned ho = W_HP + ((f >> 6) * 4 + g) * 2048 + (f & 63) * 16;
        v4u g0, u0;
        GLD4(g0, ho, ws, 0, SC1); GLD4(u0, ho, ws, 1024, SC1);
        VMW2(0, g0, u0);
        unsigned spins = 0;
        while (!allok((badtag(g0, tg) | badtag(u0, tg)) == 0u) && ++spins <= SPIN_CAP) { __builtin_amdgcn_s_sleep(4); GLD4(g0, ho, ws, 0, SC1); GLD4(u0, ho, ws, 1024, SC1); VMW2(0, g0, u0); }
        const f32x4 gf = untag(g0), uf = untag(u0);
        const float gg = rbf(tred4(gf[0], gf[1], gf[2], gf[3]));
        const float uu = tred4(uf[0], uf[1], uf[2], uf[3]);
        const float hv = rbf(gg * __builtin_amdgcn_rcpf(1.f + __expf(-gg))) * uu;
        *(LAS u16*)(L + L_ACT + g * 400 + fl * 2) = fl < 56 ? (u16)pk2(hv, hv) : (u16)0;
      }
      VMW(0);
      BAR();
      if (w < 2) {
        const int q = n16 >> 2, pp = lane & 3;
        const unsigned base = L_WD + (8 * g + q) * 256 + (((8 * w + (pp >> 1)) ^ (2 * (q | ((g & 1) << 2)))) * 16) + 8 * (pp & 1);
        const unsigned d1 = g == 3 ? 4096 : 8192;
        const v4u a0 = ldsr128(L, L_ACT + q * 400 + g * 16), a1 = ldsr128(L, L_ACT + q * 400 + g * 16 + 64);
        f32x4 fo4;
#pragma unroll
        for (int t = 0; t < 4; ++t) {
          const unsigned bt = base ^ (t * 32);
          f32x4 acc = {0.f, 0.f, 0.f, 0.f};
          acc = mfma16(a0, ldstr(L, bt, bt + 1024), acc);
          acc = mfma16(a1, ldstr(L, bt + d1, bt + d1 + 1024), acc);
          fo4[t] = acc[0];
        }
        const v4u fv = settag(fo4, tg);
        const unsigned fo = W_OUT2 + (g * 32 + 4 * (2 * qq + w) + q) * 10240 + (128 + h) * 64 + pp * 16;
        GST4(fo, fv, ws, 0, SC1);
      }
    }
    {
      const int j = bid - 128;
      unsigned off = W_OUT2 + j * 10240 + w * 1024 + lane * 16, off2 = W_OUT2 + j * 10240 + 8192 + w * 256 + n16 * 16;
      const unsigned town = __builtin_amdgcn_readfirstlane(*(LAS unsigned*)(L + L_FL + 8));
      PIN(off); PIN(off2);
      WAITRT(town, K_DGS);
      {
        const unsigned ho = W_GS + (16 * w + n16) * 128;
        v2u hv;
        unsigned sp = 0;
        for (;;) {
          GLD2(hv, ho, ws, 0, SC1);
          VMW1(0, hv);
          if (allok(hv.x == k0) || ++sp > SPIN_CAP) break;
          __builtin_amdgcn_s_sleep(6);
        }
        const unsigned now = rt32();
        unsigned age = now + 2048u - hv.y;
        age = min(age, dppu<0x128>(age)); age = min(age, dppu<0x124>(age)); age = min(age, dppu<0x4E>(age)); age = min(age, dppu<0xB1>(age));
        *(LAS unsigned*)(L + L_LS + w * 4) = now + 2048u - age;
        BAR();
        const v4u ta = ldsr128(L, L_LS), tb = ldsr128(L, L_LS + 16);
        const unsigned n4 = now + 4096u;
        const unsigned tg2 = n4 - __builtin_amdgcn_readfirstlane(min(min(min(n4 - ta.x, n4 - ta.y), min(n4 - ta.z, n4 - ta.w)), min(min(n4 - tb.x, n4 - tb.y), min(n4 - tb.z, n4 - tb.w))));
        WAITRT(tg2, K_DOUT);
      }
      v4u o, o2;
      unsigned spins = 0;
      for (;;) {
        GLD4(o, off, ws, 0, SC1); GLD4(o2, off2, ws, 0, SC1);
        VMW2(0, o, o2);
        if (allok((badtag(o, tg) | badtag(o2, tg)) == 0u) || ++spins > SPIN_CAP) break;
      }
      f32x4 s = untag(o);
      if (g == 0) s += untag(o2);
      float r = tred4(s[0], s[1], s[2], s[3]);
      r += dppf<0x128>(r); r += dppf<0x124>(r);
      *(LAS float*)(L + L_MX + (g * 4 + (lane & 3)) * 32 + w * 4) = r;
      BAR();
      if (w == 0) {
        const f32x4 s0 = *(LAS f32x4*)(L + L_MX + n16 * 32), s1 = *(LAS f32x4*)(L + L_MX + n16 * 32 + 16);
        const float t = ((s0[0] + s0[1]) + (s0[2] + s0[3])) + ((s1[0] + s1[1]) + (s1[2] + s1[3]));
        const unsigned oo = ((j >> 2) * 64 + (n16 >> 2) * 16 + (j & 3) * 4 + (lane & 3)) * 2, rb = pk2(t, t);
        if (lane < 16) GSTH(oo, rb, a.out, 0, " sc1");
        if (j == 0) {
          const v2u kv = {k0, k1};
          const unsigned z = 0;
          if (lane == 0) GST2(z, kv, ws, W_E, SC1);
        }
      }
    }
  }
}

__device__ __attribute__((used, noinline)) static void fused_decode_tail() { asm volatile("fused_decode_text_end:"); }

extern "C" void kernel_launch(void* const* d_in, const int* in_sizes, int n_in, void* d_out, int out_size, void* d_ws, size_t ws_size,
                              hipStream_t stream) {
  Args a;
  a.x = (const u16*)d_in[0]; a.ckv = (const u16*)d_in[1]; a.wq = (const u16*)d_in[2]; a.wuk = (const u16*)d_in[3];
  a.wuv = (const u16*)d_in[4]; a.wo = (const u16*)d_in[5]; a.wg = (const u16*)d_in[7]; a.wu = (const u16*)d_in[8];
  a.wd = (const u16*)d_in[9]; a.gw = (const u16*)d_in[10]; a.out = (u16*)d_out; a.ws = (unsigned char*)d_ws;
  hipLaunchKernelGGL(fused_decode, dim3(256), dim3(512), 0, stream, a);
}
```
